```python
import torch

def _softmax(x, axis=-1):
    return torch.softmax(x, dim=axis)
def _gelu(x):
    return torch.nn.functional.gelu(x, approximate="tanh")
def _silu(x):
    return torch.nn.functional.silu(x)
def _sigmoid(x):
    return torch.sigmoid(x)
def _layernorm(x, axis=-1, eps=1e-5):
    m = x.mean(dim=axis, keepdim=True)
    v = x.var(dim=axis, keepdim=True, unbiased=False)
    return (x - m) / torch.sqrt(v + eps)
def _scatter_add(tgt, idx, src, axis=0):
    return tgt.clone().index_add_(axis, idx, src)
def _segment_sum(src, seg, n):
    out = torch.zeros((n,) + src.shape[1:], dtype=src.dtype)
    return out.index_add_(0, seg, src)
def _pad(x, pw):
    flat = tuple(v for pair in reversed(pw) for v in pair)
    return torch.nn.functional.pad(x, flat)

kernel_name = 'standalone__linear_kernel_hedgehog__prompt'

def setup_inputs():

    _w = (lambda a: (lambda t: t.to(torch.bfloat16) if t.is_floating_point() else t)(torch.as_tensor(a)))
    B, S, D, H = 1, 2048, 640, 32
    Dh = D // H
    return {
        "x": _w((lambda *s: torch.randn(*s, dtype=torch.bfloat16) * 0.4)(B, S, D)),
        "wqkv": _w((lambda *s: torch.randn(*s, dtype=torch.bfloat16) * 0.4)(D, 3 * D)),
        "w_phi": _w((lambda *s: torch.randn(*s, dtype=torch.bfloat16) * 0.4)(H, Dh, Dh) * (Dh ** -0.5)),
        "wo": _w((lambda *s: torch.randn(*s, dtype=torch.bfloat16) * 0.05)(D, D)),
    }

def reference(x, wqkv, w_phi, wo):
    B, S, D = x.shape
    H = 32; Dh = D // H
    qkv = (lambda x, *d: x.permute(*d))((x @ wqkv).reshape(B, S, 3, H, Dh), 2, 0, 3, 1, 4)
    q, k, v = qkv[0], qkv[1], qkv[2]
    pq = torch.einsum("bhsd,hdf->bhsf", q, w_phi)
    pk = torch.einsum("bhsd,hdf->bhsf", k, w_phi)
    phi_q = torch.concatenate([_softmax(pq, axis=-1), _softmax(-pq, axis=-1)], axis=-1)
    phi_k = torch.concatenate([_softmax(pk, axis=-1), _softmax(-pk, axis=-1)], axis=-1)
    kv = torch.einsum("bhsf,bhsd->bhfd", phi_k, v)
    z  = torch.einsum("bhsf->bhf", phi_k)
    num = torch.einsum("bhqf,bhfd->bhqd", phi_q, kv)
    den = torch.einsum("bhqf,bhf->bhq", phi_q, z)[..., None] + 1e-6
    o = num / den
    return (lambda x, *d: x.permute(*d))(o, 0, 2, 1, 3).reshape(B, S, D) @ wo
```

```cpp
#include <hip/hip_runtime.h>
#include <hip/hip_bf16.h>

constexpr int S = 2048, D = 640, H = 32, DH = 20, N3 = 1920;
typedef unsigned short u16;
__device__ inline float b2f(u16 v) { return __uint_as_float(((unsigned)v) << 16); }
__device__ inline u16 f2b(float f) { unsigned u = __float_as_uint(f); u += 0x7fffu + ((u >> 16) & 1u); return (u16)(u >> 16); }

__global__ void k_gemm(const u16* A, const u16* B, u16* C, int M, int N, int K) {
    __shared__ float sa[16][17], sb[16][17];
    int tx = threadIdx.x & 15, ty = threadIdx.x >> 4;
    int row = blockIdx.y * 16 + ty, col = blockIdx.x * 16 + tx;
    float acc = 0.f;
    for (int k0 = 0; k0 < K; k0 += 16) {
        sa[ty][tx] = b2f(A[row * K + k0 + tx]);
        sb[ty][tx] = b2f(B[(k0 + ty) * N + col]);
        __syncthreads();
        for (int k = 0; k < 16; ++k) acc += sa[ty][k] * sb[k][tx];
        __syncthreads();
    }
    C[row * N + col] = f2b(acc);
}

__global__ void k_phi(const u16* qkv, const u16* wphi, float* phi) {
    int t = blockIdx.x * blockDim.x + threadIdx.x;
    int which = t / (S * H); int r = t % (S * H); int s = r / H, h = r % H;
    const u16* a = qkv + s * N3 + which * D + h * DH;
    float* o = phi + ((size_t)(which * S + s) * H + h) * 40;
    float mx = -1e30f, mn = 1e30f;
    for (int f = 0; f < DH; ++f) {
        float acc = 0.f;
        for (int d = 0; d < DH; ++d) acc += b2f(a[d]) * b2f(wphi[(h * DH + d) * DH + f]);
        o[f] = acc; mx = fmaxf(mx, acc); mn = fminf(mn, acc);
    }
    float s1 = 0.f, s2 = 0.f;
    for (int f = 0; f < DH; ++f) { float p = o[f]; float e1 = __expf(p - mx), e2 = __expf(mn - p); o[f] = e1; o[20 + f] = e2; s1 += e1; s2 += e2; }
    for (int f = 0; f < DH; ++f) { o[f] /= s1; o[20 + f] /= s2; }
}

__global__ void k_kv(const u16* qkv, const float* phi, float* kv) {
    int h = blockIdx.x; int t = threadIdx.x;
    if (t >= 40 * 21) return;
    int f = t / 21, d = t % 21;
    float acc = 0.f;
    for (int s = 0; s < S; ++s) {
        float p = phi[((size_t)(S + s) * H + h) * 40 + f];
        float v = d < 20 ? b2f(qkv[s * N3 + 2 * D + h * DH + d]) : 1.f;
        acc += p * v;
    }
    kv[h * 840 + t] = acc;
}

__global__ void k_o(const float* phi, const float* kv, u16* o) {
    int t = blockIdx.x * blockDim.x + threadIdx.x;
    int s = t / D, c = t % D, h = c / DH, d = c % DH;
    const float* p = phi + ((size_t)s * H + h) * 40;
    float num = 0.f, den = 0.f;
    for (int f = 0; f < 40; ++f) { num += p[f] * kv[h * 840 + f * 21 + d]; den += p[f] * kv[h * 840 + f * 21 + 20]; }
    o[t] = f2b(num / (den + 1e-6f));
}

extern "C" void kernel_launch(void* const* d_in, const int* in_sizes, int n_in,
                              void* d_out, int out_size, void* d_ws, size_t ws_size,
                              hipStream_t stream) {
    const u16* x = (const u16*)d_in[0]; const u16* wqkv = (const u16*)d_in[1];
    const u16* wphi = (const u16*)d_in[2]; const u16* wo = (const u16*)d_in[3];
    char* ws = (char*)d_ws;
    u16* qkv = (u16*)ws; float* phi = (float*)(ws + (16 << 20)); float* kv = (float*)(ws + (48 << 20)); u16* o = (u16*)(ws + (56 << 20));
    k_gemm<<<dim3(N3 / 16, S / 16), 256, 0, stream>>>(x, wqkv, qkv, S, N3, D);
    k_phi<<<2 * S * H / 256, 256, 0, stream>>>(qkv, wphi, phi);
    k_kv<<<H, 896, 0, stream>>>(qkv, phi, kv);
    k_o<<<S * D / 256, 256, 0, stream>>>(phi, kv, o);
    k_gemm<<<dim3(D / 16, S / 16), 256, 0, stream>>>(o, wo, (u16*)d_out, S, D, D);
}
```

```cpp
#define PROBE_MODE 0
#include <hip/hip_runtime.h>
#include <hip/hip_bf16.h>

constexpr int S = 2048, D = 640, H = 32, DH = 20, N3 = 1920;
typedef unsigned short u16;
__device__ inline float b2f(u16 v) { return __uint_as_float(((unsigned)v) << 16); }
__device__ inline u16 f2b(float f) { unsigned u = __float_as_uint(f); u += 0x7fffu + ((u >> 16) & 1u); return (u16)(u >> 16); }

__global__ void k_gemm(const u16* A, const u16* B, u16* C, int M, int N, int K) {
    __shared__ float sa[16][17], sb[16][17];
    int tx = threadIdx.x & 15, ty = threadIdx.x >> 4;
    int row = blockIdx.y * 16 + ty, col = blockIdx.x * 16 + tx;
    float acc = 0.f;
    for (int k0 = 0; k0 < K; k0 += 16) {
        sa[ty][tx] = b2f(A[row * K + k0 + tx]);
        sb[ty][tx] = b2f(B[(k0 + ty) * N + col]);
        __syncthreads();
        for (int k = 0; k < 16; ++k) acc += sa[ty][k] * sb[k][tx];
        __syncthreads();
    }
    C[row * N + col] = f2b(acc);
}

__global__ void k_phi(const u16* qkv, const u16* wphi, float* phi) {
    int t = blockIdx.x * blockDim.x + threadIdx.x;
    int which = t / (S * H); int r = t % (S * H); int s = r / H, h = r % H;
    const u16* a = qkv + s * N3 + which * D + h * DH;
    float* o = phi + ((size_t)(which * S + s) * H + h) * 40;
    float mx = -1e30f, mn = 1e30f;
    for (int f = 0; f < DH; ++f) {
        float acc = 0.f;
        for (int d = 0; d < DH; ++d) acc += b2f(a[d]) * b2f(wphi[(h * DH + d) * DH + f]);
        o[f] = acc; mx = fmaxf(mx, acc); mn = fminf(mn, acc);
    }
    float s1 = 0.f, s2 = 0.f;
    for (int f = 0; f < DH; ++f) { float p = o[f]; float e1 = __expf(p - mx), e2 = __expf(mn - p); o[f] = e1; o[20 + f] = e2; s1 += e1; s2 += e2; }
    for (int f = 0; f < DH; ++f) { o[f] /= s1; o[20 + f] /= s2; }
}

__global__ void k_kv(const u16* qkv, const float* phi, float* kv) {
    int h = blockIdx.x; int t = threadIdx.x;
    if (t >= 40 * 21) return;
    int f = t / 21, d = t % 21;
    float acc = 0.f;
    for (int s = 0; s < S; ++s) {
        float p = phi[((size_t)(S + s) * H + h) * 40 + f];
        float v = d < 20 ? b2f(qkv[s * N3 + 2 * D + h * DH + d]) : 1.f;
        acc += p * v;
    }
    kv[h * 840 + t] = acc;
}

__global__ void k_o(const float* phi, const float* kv, u16* o) {
    int t = blockIdx.x * blockDim.x + threadIdx.x;
    int s = t / D, c = t % D, h = c / DH, d = c % DH;
    const float* p = phi + ((size_t)s * H + h) * 40;
    float num = 0.f, den = 0.f;
    for (int f = 0; f < 40; ++f) { num += p[f] * kv[h * 840 + f * 21 + d]; den += p[f] * kv[h * 840 + f * 21 + 20]; }
    o[t] = f2b(num / (den + 1e-6f));
}

#define GAS __attribute__((address_space(1)))
#define LAS __attribute__((address_space(3)))
typedef unsigned long long u64;
__device__ __forceinline__ void dma16(const char* g, char* l) { __builtin_amdgcn_global_load_lds((const GAS unsigned*)g, (LAS unsigned*)l, 16, 0, 0); }
__global__ __launch_bounds__(256, 1) void k_probe(const char* x, const char* w, unsigned* sink) {
    extern __shared__ __attribute__((aligned(16))) char lds[];
    const int tid = threadIdx.x, lane = tid & 63, wave = __builtin_amdgcn_readfirstlane(tid >> 6);
    const int b = blockIdx.x, xcd = b & 7, r = b >> 3;
    int s = (xcd & 3) * 4 + (r & 3), hg = (xcd >> 2) * 8 + (r >> 2);
    u64 t0 = __builtin_amdgcn_s_memrealtime();
    asm volatile("s_waitcnt vmcnt(0)" ::: "memory");
    __syncthreads();
    u64 t1 = __builtin_amdgcn_s_memrealtime();
    u64 until = t1 + (t1 - t0) * 100;
    while (__builtin_amdgcn_s_memrealtime() < until) __builtin_amdgcn_s_sleep(16);
    if (t1 == 0x12345678u) sink[tid] = *(unsigned*)(lds + tid * 4);
}

extern "C" void kernel_launch(void* const* d_in, const int* in_sizes, int n_in,
                              void* d_out, int out_size, void* d_ws, size_t ws_size,
                              hipStream_t stream) {
    const u16* x = (const u16*)d_in[0]; const u16* wqkv = (const u16*)d_in[1];
    const u16* wphi = (const u16*)d_in[2]; const u16* wo = (const u16*)d_in[3];
    char* ws = (char*)d_ws;
    u16* qkv = (u16*)ws; float* phi = (float*)(ws + (16 << 20)); float* kv = (float*)(ws + (48 << 20)); u16* o = (u16*)(ws + (56 << 20));
    hipFuncSetAttribute((const void*)k_probe, hipFuncAttributeMaxDynamicSharedMemorySize, 163840);
    k_probe<<<256, 256, 163840, stream>>>((const char*)x, (const char*)wqkv, (unsigned*)(ws + (100 << 20)));
    k_gemm<<<dim3(N3 / 16, S / 16), 256, 0, stream>>>(x, wqkv, qkv, S, N3, D);
    k_phi<<<2 * S * H / 256, 256, 0, stream>>>(qkv, wphi, phi);
    k_kv<<<H, 896, 0, stream>>>(qkv, phi, kv);
    k_o<<<S * D / 256, 256, 0, stream>>>(phi, kv, o);
    k_gemm<<<dim3(D / 16, S / 16), 256, 0, stream>>>(o, wo, (u16*)d_out, S, D, D);
}
```

```cpp
#include <hip/hip_runtime.h>
#include <hip/hip_bf16.h>
#include <utility>

#define DI __device__ __forceinline__
#define GAS __attribute__((address_space(1)))
#define LAS __attribute__((address_space(3)))
#define CAS __attribute__((address_space(4)))
typedef unsigned long long u64;
typedef unsigned u2 __attribute__((ext_vector_type(2)));
typedef unsigned u4 __attribute__((ext_vector_type(4)));
typedef short s4 __attribute__((ext_vector_type(4)));
typedef short s8 __attribute__((ext_vector_type(8)));
typedef __bf16 b2 __attribute__((ext_vector_type(2)));
typedef __bf16 b8 __attribute__((ext_vector_type(8)));
typedef float f2 __attribute__((ext_vector_type(2)));
typedef float f4 __attribute__((ext_vector_type(4)));
typedef float f16v __attribute__((ext_vector_type(16)));

#define DIAG 0

constexpr unsigned WS_EPOCH = 0;
constexpr unsigned WS_FL1 = 4096;
constexpr unsigned WS_FL2 = 65536;
constexpr unsigned WS_KVP = 262144;
constexpr unsigned WS_O = 1310720;
constexpr unsigned WS_END = WS_O + 2048 * 640 * 2;
constexpr unsigned WS_DIAG = 8u << 20;

constexpr unsigned L_WKV = 0;
constexpr unsigned L_WQ = 0;
constexpr unsigned L_WO = 51200;
constexpr unsigned L_XR = 102400;
constexpr unsigned L_WPHI = 153600;
constexpr unsigned L_ZERO = 155648;
constexpr unsigned L_JUNK = 155904;
constexpr unsigned L_EPOCH = 156160;
constexpr unsigned L_KVT = 156416;
constexpr unsigned L_SIZE = 163840;

constexpr unsigned SPIN_CAP = 1u << 21;

DI u4 make_srd(const void* p, unsigned bytes) {
    const u64 a = (u64)p;
    return (u4){(unsigned)a, (unsigned)(a >> 32) & 0xffffu, bytes, 0x00020000u};
}
DI void set_m0(unsigned lds) { asm volatile("s_mov_b32 m0, %0\n\ts_nop 0" :: "s"(lds) : "memory"); }
template <int OFF> DI void dma(u4 srd, unsigned soff, unsigned voff) {
    asm volatile("buffer_load_dwordx4 %2, %0, %1 offen offset:%3 lds" :: "s"(srd), "s"(soff), "v"(voff), "i"(OFF) : "memory");
}
template <int PH> DI void dma_piece(unsigned lds, u4 srd, unsigned soff, unsigned voff) {
    if (PH == 0) set_m0(lds);
    if (PH == 4) set_m0(lds + 4096);
    dma<(PH & 3) * 1024>(srd, soff, voff);
}
DI void touch(unsigned lds, u4 srd, unsigned voff) {
    asm volatile("s_mov_b32 m0, %0\n\ts_nop 0\n\tbuffer_load_dword %2, %1, 0 offen lds" :: "s"(lds), "s"(srd), "v"(voff) : "memory");
}
template <class F, int... I> DI void static_for_impl(F&& f, std::integer_sequence<int, I...>) { (f(std::integral_constant<int, I>{}), ...); }
template <int N, class F> DI void static_for(F&& f) { static_for_impl(f, std::make_integer_sequence<int, N>{}); }
#define SB() __builtin_amdgcn_sched_barrier(0)
#define WAIT_VM(n) asm volatile("s_waitcnt vmcnt(" #n ")" ::: "memory")
#define WAIT_VM_BAR(n) asm volatile("s_waitcnt vmcnt(" #n ") lgkmcnt(0)\n\ts_barrier" ::: "memory")
#define WAIT_LGKM() asm volatile("s_waitcnt lgkmcnt(0)" ::: "memory")
#define LGKM_BAR() asm volatile("s_waitcnt lgkmcnt(0)\n\ts_barrier" ::: "memory")

DI s4 tr_read(LAS char* p) { return __builtin_amdgcn_ds_read_tr16_b64_v4i16((LAS s4*)p); }
DI b8 tr_frag(LAS char* p0, LAS char* p1) {
    const s4 lo = tr_read(p0), hi = tr_read(p1);
    return __builtin_bit_cast(b8, __builtin_shufflevector(lo, hi, 0, 1, 2, 3, 4, 5, 6, 7));
}
DI unsigned pack2(float a, float b) { return __builtin_bit_cast(unsigned, __builtin_convertvector((f2){a, b}, b2)); }
DI b8 as_frag(unsigned a, unsigned b, unsigned c, unsigned d) { return __builtin_bit_cast(b8, (u4){a, b, c, d}); }
#define DEF_MFMA16(name, ca, cb)                                                                                          \
    template <class A, class B> DI void name(bool first, f4& acc, const A& a, const B& b) {                              \
        if (first) asm volatile("v_mfma_f32_16x16x32_bf16 %0, %1, %2, 0" : "=&v"(acc) : ca(a), cb(b));                    \
        else asm volatile("v_mfma_f32_16x16x32_bf16 %0, %1, %2, %0" : "+v"(acc) : ca(a), cb(b));                          \
    }
DEF_MFMA16(mfma16_vv, "v", "v")
DEF_MFMA16(mfma16_va, "v", "a")
DEF_MFMA16(mfma16_av, "a", "v")
DI void mfma32_pad(bool first, f16v& acc, const b8& a, const b8& b) {
    if (first) asm volatile("s_nop 1\n\tv_mfma_f32_32x32x16_bf16 %0, %1, %2, 0" : "=&v"(acc) : "v"(a), "v"(b));
    else asm volatile("s_nop 1\n\tv_mfma_f32_32x32x16_bf16 %0, %1, %2, %0" : "+v"(acc) : "v"(a), "v"(b));
}
DI void mfma32k8_pad(f16v& acc, const s4& a, const s4& b) {
    asm volatile("s_nop 1\n\tv_mfma_f32_32x32x8_bf16 %0, %1, %2, %0" : "+v"(acc) : "v"(a), "v"(b));
}
template <class T> DI void pin(T& a) { asm volatile("" : "+v"(a)); }
template <class T, class... R> DI void pin(T& a, R&... r) { pin(a); pin(r...); }
DI void landed(const b8& a, const b8& b) { asm volatile("" :: "a"(a), "a"(b) : "memory"); }
template <int N> DI void wait_vm() { asm volatile("s_waitcnt vmcnt(%0)" :: "i"(N) : "memory"); }

struct PhiW { b8 a0[2], a1[2]; s4 a2[2]; };

template <class F> DI void feature_map(const PhiW& w, const unsigned (&in)[10], unsigned (&out)[20], F&& hook) {
    const b8 b0 = as_frag(in[0], in[1], in[2], in[3]), b1 = as_frag(in[4], in[5], in[6], in[7]);
    const s4 b2_ = __builtin_bit_cast(s4, (u2){in[8], in[9]});
    f16v d0, d1;
    mfma32_pad(true, d0, w.a0[0], b0);
    mfma32_pad(false, d0, w.a1[0], b1);
    mfma32k8_pad(d0, w.a2[0], b2_);
    mfma32_pad(true, d1, w.a0[1], b0);
    mfma32_pad(false, d1, w.a1[1], b1);
    mfma32k8_pad(d1, w.a2[1], b2_);
    hook(std::integral_constant<int, 0>{});
    asm volatile("s_nop 7" : "+v"(d0));
    float p[20];
#pragma unroll
    for (int r = 0; r < 16; ++r) p[r] = d0[r];
    float mx = p[0], mn = p[0];
#pragma unroll
    for (int r = 1; r < 15; ++r) { mx = __builtin_elementwise_maximum(mx, p[r]); mn = __builtin_elementwise_minimum(mn, p[r]); }
    asm volatile("" : "+v"(mx), "+v"(mn), "+v"(d1));
#pragma unroll
    for (int r = 16; r < 20; ++r) p[r] = d1[r - 16];
#pragma unroll
    for (int r = 15; r < 20; ++r) { mx = __builtin_elementwise_maximum(mx, p[r]); mn = __builtin_elementwise_minimum(mn, p[r]); }
    constexpr float L = 1.4426950408889634f;
    const f2 cp = {-mx * L, -mx * L}, cn = {mn * L, mn * L};
    f2 e[10], f[10];
    static_for<10>([&](auto mc) {
        constexpr int m = mc;
        if (m == 0) { SB(); hook(std::integral_constant<int, 1>{}); SB(); }
        if (m == 4) { SB(); hook(std::integral_constant<int, 2>{}); SB(); }
        if (m == 8) { SB(); hook(std::integral_constant<int, 3>{}); SB(); }
        const f2 x = {p[2 * m], p[2 * m + 1]};
        const f2 a = __builtin_elementwise_fma(x, (f2){L, L}, cp), b = __builtin_elementwise_fma(x, (f2){-L, -L}, cn);
        e[m] = (f2){__builtin_amdgcn_exp2f(a[0]), __builtin_amdgcn_exp2f(a[1])};
        f[m] = (f2){__builtin_amdgcn_exp2f(b[0]), __builtin_amdgcn_exp2f(b[1])};
    });
    f2 se = (e[0] + e[1]) + (e[2] + e[3]) + ((e[4] + e[5]) + (e[6] + e[7])) + (e[8] + e[9]);
    f2 sf = (f[0] + f[1]) + (f[2] + f[3]) + ((f[4] + f[5]) + (f[6] + f[7])) + (f[8] + f[9]);
    const float re = __builtin_amdgcn_rcpf(se[0] + se[1]), rf = __builtin_amdgcn_rcpf(sf[0] + sf[1]);
    SB(); hook(std::integral_constant<int, 4>{}); SB();
#pragma unroll
    for (int m = 0; m < 10; ++m) {
        const f2 a = e[m] * (f2){re, re}, b = f[m] * (f2){rf, rf};
        out[m] = pack2(a[0], a[1]);
        out[10 + m] = pack2(b[0], b[1]);
    }
}

DI u64 load_flag(const GAS u64* p) { return __hip_atomic_load(p, __ATOMIC_RELAXED, __HIP_MEMORY_SCOPE_AGENT); }
DI void wait_flags(const GAS u64* p, u64 epoch, u64 v) {
#pragma unroll 1
    for (unsigned spins = 0; spins < SPIN_CAP && !__all(v == epoch); ++spins) v = load_flag(p);
    asm volatile("" ::: "memory");
}
DI void set_flag(GAS u64* p, u64 epoch) { __hip_atomic_store(p, epoch, __ATOMIC_RELAXED, __HIP_MEMORY_SCOPE_AGENT); }

#define STAMP(i) do { } while (0)

__global__ __launch_bounds__(256, 1) void hedgehog_fused(const char* __restrict__ x, const char* __restrict__ wqkv,
                                                         const char* __restrict__ wphi, const char* __restrict__ wo,
                                                         char* __restrict__ out, char* __restrict__ ws) {
    __shared__ __attribute__((aligned(16))) char lds_raw[L_SIZE];
    LAS char* const lds = (LAS char*)lds_raw;
    const unsigned lb = (unsigned)(size_t)lds;
    STAMP(0);
    const unsigned b = blockIdx.x, xcd = b & 7, r = b >> 3;
    {
        u64 pc;
        asm volatile("s_getpc_b64 %0" : "=s"(pc));
        if (threadIdx.x < 8) touch(lb + L_JUNK, make_srd((const void*)(pc & ~127ull), KERNEL_BYTES), (r * 8 + threadIdx.x) * 128);
    }
    SB();
    const unsigned l = threadIdx.x & 63, w = __builtin_amdgcn_readfirstlane(threadIdx.x >> 6);
    const unsigned g = l >> 4, i = l & 15, h = l >> 5, c = l & 31, q = (l >> 2) & 3, p = l & 3, g1 = g & 1;
    const unsigned s = (xcd & 1) * 8 + (r & 7), hg = (xcd >> 1) * 4 + (r >> 3);
    const unsigned rt = xcd * 4 + (r & 3), ct = r >> 2;
    const unsigned rot = ((3 * s + hg) % 10) * 2;
    auto kstep = [&](unsigned it) { const unsigned k = rot + it; return k >= 20 ? k - 20 : k; };

    unsigned xo[4], wko[5];
#pragma unroll
    for (int m = 0; m < 4; ++m) {
        const unsigned row = 8 * m + (l >> 3);
        xo[m] = (s * 128 + 32 * w + row) * 1280 + (((l & 7) ^ ((row >> 1) & 7)) << 4) - 1024 * m;
    }
    asm volatile("" :: "s"(x), "s"(wqkv), "s"(wphi), "s"(wo), "s"(out), "s"(ws));
    STAMP(15);
    const u4 sx = make_srd(x, 2048 * 1280), sw = make_srd(wqkv, 640 * 3840), sp = make_srd(wphi, 32 * 800), so = make_srd(wo, 640 * 1280);
    const unsigned xr = L_XR + w * 12288;
    auto dma_x = [&](unsigned j, auto mc) { dma_piece<mc>(lb + xr + (j % 3) * 4096, sx, kstep(2 * j) * 64, xo[mc]); };
    static_for<4>([&](auto m) { dma_x(0, m); });
    static_for<4>([&](auto m) { dma_x(1, m); });
    SB();
#pragma unroll
    for (int ph = 0; ph < 5; ++ph) {
        const unsigned n = 64 * ph + l, u = n / 5, ch = n - 5 * u;
        const unsigned row = 16 * (u >> 5) + 8 * ((u >> 2) & 1) + 4 * ((u >> 4) & 1) + (u & 3);
        wko[ph] = row * 3840 + (1 + ((u >> 3) & 1)) * 1280 + hg * 80 + ch * 16 - (ph < 4 ? 1024 * ph : 0);
    }
    auto dma_wkv = [&](unsigned cc, auto ph) { dma_piece<ph>(lb + L_WKV + (4 * cc + w) * 5120, sw, kstep(4 * cc + w) * 122880, wko[ph]); };
    static_for<5>([&](auto ph) { dma_wkv(0, ph); });
    if (w < 2) dma_piece<0>(lb + L_WPHI + w * 1024, sp, hg * 1600 + w * 1024, l * 16);
    else if (w == 2) touch(lb + L_EPOCH, make_srd(ws, WS_END), l < 2 ? WS_EPOCH + 4 * l : (l & 3) << 20);
    else touch(lb + L_JUNK, make_srd(out, 2048 * 1280), rt * 81920 + (l & 1) * 81916);
    static_for<5>([&](auto ph) { dma_wkv(1, ph); });
    static_for<4>([&](auto m) { dma_x(2, m); });
    SB();
    if (l < 16) *(LAS unsigned*)(lds + L_ZERO + l * 4) = 0;

    unsigned kvt_dst[2];
#pragma unroll
    for (int pr = 0; pr < 2; ++pr) {
        const unsigned bi = 2 * pr + h, blk = (bi == 3 && w == 3) ? 14 : w + 4 * bi;
        const unsigned ci = 16 * blk + i, hh = ci >= 120, cc = ci - 120 * hh;
        const bool t1 = cc >= 80;
        const unsigned m = t1 ? cc - 80 : cc, sh = t1 ? 3 : 4, nfull = t1 ? 32 : 64;
        const bool f2_ = m >= nfull;
        const unsigned idx = f2_ ? 2 * (m - nfull) + g1 : m;
        const unsigned d = (t1 ? 16 : 0) + (idx & ((1u << sh) - 1)), f0 = (f2_ ? 32 : 16 * g1) + 4 * (idx >> sh);
        kvt_dst[pr] = L_KVT + hh * 1920 + d * 80 + f0 * 2;
    }
    unsigned wqo[5], woo[5];
#pragma unroll
    for (int m = 0; m < 5; ++m) {
        const unsigned ph = (w + 4 * m) % 5;
        const unsigned n = 64 * ph + l, u = n / 5, ch = n - 5 * u;
        const unsigned row = 32 * (u >> 5) + 16 * ((u >> 4) & 1) + 8 * ((u >> 3) & 1) + 4 * (u & 1) + ((u >> 1) & 3);
        wqo[m] = row * 3840 + hg * 80 + ch * 16;
    }
#pragma unroll
    for (int ph = 0; ph < 5; ++ph) {
        const unsigned n = 64 * ph + l, u = n / 10, ch = n - 10 * u;
        const unsigned row = 16 * (u >> 4) + 8 * ((u >> 2) & 1) + 4 * ((u >> 3) & 1) + (u & 3);
        woo[ph] = row * 1280 + ct * 160 + ch * 16 - (ph < 4 ? 1024 * ph : 0);
    }
    unsigned k0a, k1a, k0s, k1s;
    {
        const unsigned a_ = c >> 3, hv = (c >> 2) & 1, bb = c & 3;
        const unsigned d0 = 4 * h + 4 * a_ + bb, d1 = a_ ? 20 + bb : (h ? bb : 16 + bb);
        const bool v0 = hv == h, v1 = hv == h && a_ < 2;
        k0a = v0 ? L_KVT + h * 1920 + d0 * 80 : L_ZERO; k0s = v0 ? 16 : 0;
        k1a = v1 ? L_KVT + h * 1920 + d1 * 80 : L_ZERO; k1s = v1 ? 16 : 0;
    }
    asm volatile("" : "+v"(kvt_dst[0]), "+v"(kvt_dst[1]), "+v"(k0a), "+v"(k1a), "+v"(k0s), "+v"(k1s));
    asm volatile("" : "+v"(wqo[0]), "+v"(wqo[1]), "+v"(wqo[2]), "+v"(wqo[3]), "+v"(wqo[4]));
    asm volatile("" : "+v"(woo[0]), "+v"(woo[1]), "+v"(woo[2]), "+v"(woo[3]), "+v"(woo[4]));
    auto dma_wq = [&](auto nc) {
        const unsigned m = 4 * nc + w, pr = m / 5, ph = m - 5 * pr, pair = pr > 9 ? 9 : pr;
        dma_piece<0>(lb + L_WQ + pair * 5120 + ph * 1024, sw, kstep(2 * pair) * 122880, wqo[nc % 5]);
    };
    auto dma_wo = [&](auto nc) { const unsigned s2 = 4 * (nc / 5) + w; dma_piece<nc % 5>(lb + L_WO + s2 * 5120, so, s2 * 40960, woo[nc % 5]); };

    b8 xf[20][2];
    f4 acc[2][5];
    LAS char* const xrd0 = lds + xr + i * 128 + ((g ^ (i >> 1)) << 4);
    LAS char* const xrd1 = lds + xr + i * 128 + (((4 + g) ^ (i >> 1)) << 4);
    LAS char* const wrd = lds + L_WKV + (h * 32 + (p & 1) * 8 + g1 * 4 + q) * 80 + (p >> 1) * 40;
    auto w_frag = [&](int it, int t) { return tr_frag(wrd + it * 5120 + 8 * t, wrd + it * 5120 + 1280 + 8 * t); };
    PhiW pwk, pwq;
    u64 epoch = 0;
    b8 wa[5], wb[5];
    static_for<10>([&](auto jc) {
        constexpr int j = jc;
        if (j == 0) WAIT_VM_BAR(9); else if (j == 8) WAIT_VM_BAR(8); else if (j == 9) WAIT_VM(8); else if (j == 1) { } else if (j & 1) WAIT_VM(13); else WAIT_VM_BAR(13);
        if (j == 0) STAMP(1);
#pragma unroll
        for (int t = 0; t < 2; ++t) xf[2 * j][t] = *(LAS b8*)(xrd0 + (j % 3) * 4096 + t * 2048);
        if ((j & 1) == 0) {
#pragma unroll
            for (int t = 0; t < 5; ++t) wa[t] = w_frag(2 * j, t);
        }
#pragma unroll
        for (int t = 0; t < 2; ++t) xf[2 * j + 1][t] = *(LAS b8*)(xrd1 + (j % 3) * 4096 + t * 2048);
        SB();
        static_for<5>([&](auto t) {
            wb[t] = w_frag(2 * j + 1, t);
            mfma16_va(j == 0, acc[0][t], wa[t], xf[2 * j][0]);
            mfma16_va(j == 0, acc[1][t], wa[t], xf[2 * j][1]);
            SB();
        });
        landed(xf[2 * j + 1][0], xf[2 * j + 1][1]);
        auto refill = [&](auto nc) {
            constexpr int n = nc;
            if constexpr (j < 7) {
                if constexpr (j & 1) { if constexpr (n < 5) dma_wkv((j + 3) / 2, nc); else if constexpr (n < 9) dma_x(j + 3, std::integral_constant<int, (n < 9 ? n - 5 : 0)>{}); }
                else if constexpr (n < 4) dma_x(j + 3, nc);
            } else if constexpr (j < 9 && n < 4) dma_wq(std::integral_constant<int, 4 * (j - 7) + n>{});
        };
        static_for<5>([&](auto t) {
            if ((j & 1) == 0) wa[t] = w_frag(2 * j + 2, t);
            mfma16_va(false, acc[0][t], wb[t], xf[2 * j + 1][0]);
            refill(std::integral_constant<int, 2 * t>{});
            mfma16_va(false, acc[1][t], wb[t], xf[2 * j + 1][1]);
            refill(std::integral_constant<int, 2 * t + 1>{});
            SB();
        });
        if (j == 1) {
            const u2 ew = *(LAS u2*)(lds + L_EPOCH);
            epoch = (((u64)(unsigned)__builtin_amdgcn_readfirstlane(ew[1]) << 32) | (unsigned)__builtin_amdgcn_readfirstlane(ew[0])) * 6364136223846793005ull + 1442695040888963407ull;
            const unsigned a_ = 2 * g1 + (p >> 1);
#pragma unroll
            for (int t = 0; t < 2; ++t) {
                const bool valid = (p & 1) == h && (t == 0 || a_ == 0);
                const unsigned base = L_WPHI + h * 800 + q * 40 + (16 * t + 4 * a_) * 2;
                s4 fk[5], fq[5];
#pragma unroll
                for (int m = 0; m < 5; ++m) fk[m] = tr_read(lds + (valid ? base + m * 160 : L_ZERO));
#pragma unroll
                for (int m = 0; m < 5; ++m) fq[m] = tr_read(lds + (valid ? base + (h ? (m + 1) % 5 : m) * 160 : L_ZERO));
                pwk.a0[t] = __builtin_bit_cast(b8, __builtin_shufflevector(fk[0], fk[1], 0, 1, 2, 3, 4, 5, 6, 7));
                pwk.a1[t] = __builtin_bit_cast(b8, __builtin_shufflevector(fk[2], fk[3], 0, 1, 2, 3, 4, 5, 6, 7));
                pwk.a2[t] = fk[4];
                pwq.a0[t] = __builtin_bit_cast(b8, __builtin_shufflevector(fq[0], fq[1], 0, 1, 2, 3, 4, 5, 6, 7));
                pwq.a1[t] = __builtin_bit_cast(b8, __builtin_shufflevector(fq[2], fq[3], 0, 1, 2, 3, 4, 5, 6, 7));
                pwq.a2[t] = fq[4];
            }
            SB();
        }
    });
    STAMP(2);

    {
        unsigned kk[10], vv[10], ph[20];
        asm volatile("" : "+v"(acc[0][0]), "+v"(acc[1][0]), "+v"(acc[0][1]), "+v"(acc[1][1]), "+v"(acc[0][2]), "+v"(acc[1][2]));
        static_for<10>([&](auto mc) {
            constexpr int m = mc;
            if (m == 6) { pin(kk[0], kk[1], kk[2], kk[3], kk[4], kk[5]); pin(acc[0][3], acc[1][3], acc[0][4], acc[1][4]); }
            const unsigned a0 = pack2(acc[0][m >> 1][2 * (m & 1)], acc[0][m >> 1][2 * (m & 1) + 1]);
            const unsigned a1 = pack2(acc[1][m >> 1][2 * (m & 1)], acc[1][m >> 1][2 * (m & 1) + 1]);
            const auto sw2 = __builtin_amdgcn_permlane16_swap(a0, a1, false, false);
            kk[m] = sw2[0]; vv[m] = sw2[1];
        });
        LAS u4* const vrow = (LAS u4*)(lds + xr + 5120 + l * 48);
        feature_map(pwk, kk, ph, [&](auto n) {
            if (n == 0) {
                vrow[0] = (u4){vv[0], vv[1], vv[2], vv[3]};
                vrow[1] = (u4){vv[4], vv[5], vv[6], vv[7]};
                vrow[2] = (u4){vv[8], vv[9], 0x00003f80u, 0u};
            }
            dma_wq(std::integral_constant<int, 8 + n>{});
        });
        LAS u4* const prow = (LAS u4*)(lds + xr + l * 80);
#pragma unroll
        for (int m = 0; m < 5; ++m) prow[m] = (u4){ph[4 * m], ph[4 * m + 1], ph[4 * m + 2], ph[4 * m + 3]};
    }
    LGKM_BAR();
    STAMP(3);
    const __amdgpu_buffer_rsrc_t rws = __builtin_amdgcn_make_buffer_rsrc(ws, 0, WS_END, 0x00020000);
    {
        const unsigned hh = w >> 1, tt = w & 1, rho = hh * 32 + 16 * h + 2 * (4 * g1 + q);
        LAS char* const pa = lds + L_XR + rho * 80 + 8 * p;
        LAS char* const pb = lds + L_XR + 5120 + rho * 48 + 32 * tt + 8 * p;
        b8 fa[4][3], fb[4];
#pragma unroll
        for (int st = 0; st < 4; ++st) {
            fb[st] = tr_frag(pb + st * 12288, pb + st * 12288 + 48);
#pragma unroll
            for (int f = 0; f < 3; ++f) fa[st][f] = tr_frag(pa + st * 12288 + 32 * f, pa + st * 12288 + 80 + 32 * f);
        }
        SB();
        f4 kv[3];
        static_for<4>([&](auto st) { static_for<3>([&](auto f) { mfma16_vv(st == 0, kv[f], fa[st][f], fb[st]); }); });
        const unsigned rec = WS_KVP + (hg * 16 + s) * 3840 + hh * 1920 + tt * 1280;
        const unsigned slot = tt ? 8 * g + i : l;
        const bool on = tt == 0 || i < 8;
        const unsigned big = 0x7ffffff0u;
        unsigned o0 = on ? slot * 16 : big, o1 = (on && g < 2) ? (tt ? 512 : 1024) + slot * 8 : big;
        asm volatile("s_nop 7" : "+v"(kv[0]), "+v"(kv[1]), "+v"(kv[2]), "+v"(o0), "+v"(o1));
        __builtin_amdgcn_raw_buffer_store_b128((u4){pack2(kv[0][0], kv[0][1]), pack2(kv[0][2], kv[0][3]), pack2(kv[1][0], kv[1][1]), pack2(kv[1][2], kv[1][3])}, rws, o0, rec, 16);
        __builtin_amdgcn_raw_buffer_store_b64((u2){pack2(kv[2][0], kv[2][1]), pack2(kv[2][2], kv[2][3])}, rws, o1, rec, 16);
    }
    WAIT_VM_BAR(2);
    STAMP(4);

    f4 aq[2][3];
    const GAS u64* const fl1 = (const GAS u64*)(ws + WS_FL1 + (hg * 16 + (l >> 2)) * 128 + (l & 3) * 8);
    {
        LAS char* const qb = lds + L_WQ + (16 * h + 2 * (4 * g1 + q)) * 80;
        LAS char* const q01 = qb + (p < 2 ? 8 * p : 32 + 8 * p);
        LAS char* const q2 = qb + ((p == 1 || p == 2) ? 40 : 32);
        auto q_frag = [&](int it, int t) { LAS char* const a = (t < 2 ? q01 + 16 * t : q2) + it * 2560; return tr_frag(a, a + 80); };
        b8 qf[3][3];
#pragma unroll
        for (int it = 0; it < 2; ++it)
#pragma unroll
            for (int t = 0; t < 3; ++t) qf[it][t] = q_frag(it, t);
        SB();
        static_for<20>([&](auto itc) {
            constexpr int it = itc;
            if (it == FLAG1_IT) {
                wait_vm<(FLAG1_IT < WO_IN_Q ? FLAG1_IT : WO_IN_Q)>();
                if (l == 0) set_flag((GAS u64*)(ws + WS_FL1 + (hg * 16 + s) * 128 + w * 8), epoch);
                SB();
            }
            static_for<3>([&](auto t) {
                if (it + 2 < 20) qf[(it + 2) % 3][t] = q_frag(it + 2, t);
                mfma16_va(it == 0, aq[0][t], qf[it % 3][t], xf[it][0]);
                if constexpr (t == 0 && it < WO_IN_Q) dma_wo(itc);
                mfma16_va(it == 0, aq[1][t], qf[it % 3][t], xf[it][1]);
                SB();
            });
        });
    }
    STAMP(5);
    u64 fv = load_flag(fl1);
    SB();
    u4 part[4][4];
    unsigned phq[20];
    {
        unsigned qq[10];
        asm volatile("s_nop 6" : "+v"(aq[0][0]), "+v"(aq[1][0]), "+v"(aq[0][1]), "+v"(aq[1][1]), "+v"(aq[0][2]), "+v"(aq[1][2]));
#pragma unroll
        for (int t = 0; t < 3; ++t)
#pragma unroll
            for (int e = 0; e < 2; ++e) {
                const auto sw2 = __builtin_amdgcn_permlane16_swap(pack2(aq[0][t][2 * e], aq[0][t][2 * e + 1]), pack2(aq[1][t][2 * e], aq[1][t][2 * e + 1]), false, false);
                qq[4 * t + e] = sw2[0];
                if (t < 2) qq[4 * t + 2 + e] = sw2[1];
            }
        feature_map(pwq, qq, phq, [&](auto n) {
            if constexpr (n == GATHER_HOOK) {
                wait_flags(fl1, epoch, fv);
                STAMP(6);
#pragma unroll
                for (int bi = 0; bi < 4; ++bi) {
                    const unsigned blk = (bi == 3 && w == 3) ? 14 : w + 4 * bi;
#pragma unroll
                    for (int st = 0; st < 4; ++st)
                        part[bi][st] = __builtin_amdgcn_raw_buffer_load_b128(rws, g * 3840 + i * 16, WS_KVP + (hg * 16 + 4 * st) * 3840 + blk * 256, 16);
                }
            }
            if constexpr (n == 4) static_for<25 - WO_IN_Q>([&](auto m) { dma_wo(std::integral_constant<int, WO_IN_Q + m>{}); });
        });
#pragma unroll
        for (int m = 0; m < 20; m += 4) asm volatile("" : "+v"(phq[m]), "+v"(phq[m + 1]), "+v"(phq[m + 2]), "+v"(phq[m + 3]));
    }
    SB();
    STAMP(7);
    {
        b8 sel[2];
#pragma unroll
        for (int e = 0; e < 2; ++e) {
            u4 v;
#pragma unroll
            for (int m = 0; m < 4; ++m) v[m] = i == 8 * e + 2 * m ? 0x00003f80u : i == 8 * e + 2 * m + 1 ? 0x3f800000u : 0u;
            sel[e] = __builtin_bit_cast(b8, v);
        }
        asm volatile("s_nop 1" : "+v"(sel[0]), "+v"(sel[1]));
        f4 sum[2];
        static_for<2>([&](auto pr) { static_for<8>([&](auto n) { mfma16_vv(n == 0, sum[pr], sel[n / 4], part[2 * pr + n / 4][n % 4]); }); });
        asm volatile("" : "+v"(sum[0]));
        *(LAS u2*)(lds + kvt_dst[0]) = (u2){pack2(sum[0][0], sum[0][1]), pack2(sum[0][2], sum[0][3])};
        asm volatile("s_nop 4" : "+v"(sum[1]) :: "memory");
        *(LAS u2*)(lds + kvt_dst[1]) = (u2){pack2(sum[1][0], sum[1][1]), pack2(sum[1][2], sum[1][3])};
    }
    WAIT_VM_BAR(0);
    STAMP(8);
    {
        b8 ka[5], kb[5];
#pragma unroll
        for (int st = 0; st < 5; ++st) { kb[st] = *(LAS b8*)(lds + k1a + st * k1s); ka[st] = *(LAS b8*)(lds + k0a + st * k0s); }
        SB();
        f16v n0, n1;
        static_for<5>([&](auto st) {
            const b8 bq = as_frag(phq[4 * st], phq[4 * st + 1], phq[4 * st + 2], phq[4 * st + 3]);
            mfma32_pad(st == 0, n1, kb[st], bq);
        });
        static_for<5>([&](auto st) {
            const b8 bq = as_frag(phq[4 * st], phq[4 * st + 1], phq[4 * st + 2], phq[4 * st + 3]);
            mfma32_pad(st == 0, n0, ka[st], bq);
        });
        asm volatile("" : "+v"(n1));
        float inv = __builtin_amdgcn_rcpf(n1[4] + 1e-6f);
        unsigned o[10];
        o[8] = pack2(n1[0] * inv, n1[1] * inv);
        o[9] = pack2(n1[2] * inv, n1[3] * inv);
        const unsigned ob = WS_O + (s * 8 + 2 * w) * 20480 + hg * 1280;
        const unsigned vo = (c >> 4) * 20480 + i * 16;
        __builtin_amdgcn_raw_buffer_store_b64((u2){o[8], o[9]}, rws, vo + 512 + h * 8, ob, 16);
        asm volatile("s_nop 3" : "+v"(n0), "+v"(inv) :: "memory");
#pragma unroll
        for (int m = 0; m < 8; ++m) o[m] = pack2(n0[2 * m] * inv, n0[2 * m + 1] * inv);
        __builtin_amdgcn_raw_buffer_store_b128((u4){o[0], o[1], o[2], o[3]}, rws, vo + h * 768, ob, 16);
        __builtin_amdgcn_raw_buffer_store_b128((u4){o[4], o[5], o[6], o[7]}, rws, vo + h * 768 + 256, ob, 16);
        SB();
        asm volatile("s_nop 1" ::: "memory");
        SB();
    }
    STAMP(9);

    LAS char* const word = lds + L_WO + (16 * h + 4 * g1 + q) * 160 + 8 * p;
    auto o_frag = [&](int s2, int t) { return tr_frag(word + s2 * 5120 + 32 * t, word + s2 * 5120 + 1280 + 32 * t); };
    b8 wf[20][5];
    static_for<16>([&](auto s2) {
        if (s2 == PRE_BEFORE_FLAG) {
            WAIT_VM(0);
            if (l == 0) set_flag((GAS u64*)(ws + WS_FL2 + ((s * 4 + w) * 16 + hg) * 128), epoch);
            STAMP(10);
        }
#pragma unroll
        for (int t = 0; t < 5; ++t) wf[s2][t] = o_frag(s2, t);
        SB();
    });
    const GAS u64* const fl2 = (const GAS u64*)(ws + WS_FL2 + ((rt * 2 + (w >> 1)) * 16 + i) * 128);
    wait_flags(fl2, epoch, load_flag(fl2));
    STAMP(11);
    u4 of[20];
#pragma unroll
    for (int s2 = 0; s2 < 20; ++s2) of[s2] = __builtin_amdgcn_raw_buffer_load_b128(rws, l * 16, WS_O + (rt * 4 + w) * 20480 + s2 * 1024, 16);
    SB();
    f4 ao[5];
    static_for<20>([&](auto s2c) {
        constexpr int s2 = s2c;
        static_for<5>([&](auto t) {
            if constexpr (s2 >= 13 && s2 < 17) wf[s2 + 3][t] = o_frag(s2 + 3, t);
            if constexpr (s2 < 12) mfma16_av(s2 == 0, ao[t], wf[s2][t], of[s2]);
            else mfma16_vv(false, ao[t], wf[s2][t], of[s2]);
            SB();
        });
    });
    STAMP(12);
    {
        GAS char* const orow = (GAS char*)out + (rt * 64 + 16 * w + i) * 1280 + ct * 160;
        asm volatile("s_nop 3" : "+v"(ao[0]), "+v"(ao[1]));
        static_for<2>([&](auto tc) {
            constexpr int t = 2 * tc;
            if (t == 2) { SB(); pin(ao[2], ao[3], ao[4]); }
            const auto lo = __builtin_amdgcn_permlane16_swap(pack2(ao[t][0], ao[t][1]), pack2(ao[t + 1][0], ao[t + 1][1]), false, false);
            const auto hi = __builtin_amdgcn_permlane16_swap(pack2(ao[t][2], ao[t][3]), pack2(ao[t + 1][2], ao[t + 1][3]), false, false);
            *(GAS u4*)(orow + 32 * t + 32 * g1 + 16 * h) = (u4){lo[0], hi[0], lo[1], hi[1]};
        });
        *(GAS u2*)(orow + 128 + 8 * g) = (u2){pack2(ao[4][0], ao[4][1]), pack2(ao[4][2], ao[4][3])};
    }
    if (b == 0 && threadIdx.x == 0) set_flag((GAS u64*)(ws + WS_EPOCH), epoch);
}

extern "C" void kernel_launch(void* const* d_in, const int* in_sizes, int n_in, void* d_out, int out_size, void* d_ws,
                              size_t ws_size, hipStream_t stream) {
    hedgehog_fused<<<256, 256, 0, stream>>>((const char*)d_in[0], (const char*)d_in[1], (const char*)d_in[2],
                                            (const char*)d_in[3], (char*)d_out, (char*)d_ws);
}
```

```cpp
#define DIAG 1
#define PROBE_K 15
#define PROBE_K0 0
#include <hip/hip_runtime.h>
#include <hip/hip_bf16.h>
#include <utility>

#define DI __device__ __forceinline__
#define GAS __attribute__((address_space(1)))
#define LAS __attribute__((address_space(3)))
#define CAS __attribute__((address_space(4)))
typedef unsigned long long u64;
typedef unsigned u2 __attribute__((ext_vector_type(2)));
typedef unsigned u4 __attribute__((ext_vector_type(4)));
typedef short s4 __attribute__((ext_vector_type(4)));
typedef short s8 __attribute__((ext_vector_type(8)));
typedef __bf16 b2 __attribute__((ext_vector_type(2)));
typedef __bf16 b8 __attribute__((ext_vector_type(8)));
typedef float f2 __attribute__((ext_vector_type(2)));
typedef float f4 __attribute__((ext_vector_type(4)));
typedef float f16v __attribute__((ext_vector_type(16)));

constexpr unsigned WS_EPOCH = 0;
constexpr unsigned WS_FL1 = 4096;
constexpr unsigned WS_FL2 = 65536;
constexpr unsigned WS_KVP = 262144;
constexpr unsigned WS_O = 1310720;
constexpr unsigned WS_END = WS_O + 2048 * 640 * 2;
constexpr unsigned WS_DIAG = 8u << 20;

constexpr unsigned L_WKV = 0;
constexpr unsigned L_WQ = 0;
constexpr unsigned L_WO = 51200;
constexpr unsigned L_XR = 102400;
constexpr unsigned L_WPHI = 153600;
constexpr unsigned L_ZERO = 155648;
constexpr unsigned L_JUNK = 155904;
constexpr unsigned L_EPOCH = 156160;
constexpr unsigned L_KVT = 156416;
constexpr unsigned L_SIZE = 163840;

constexpr unsigned SPIN_CAP = 1u << 21;

DI u4 make_srd(const void* p, unsigned bytes) {
    const u64 a = (u64)p;
    return (u4){(unsigned)a, (unsigned)(a >> 32) & 0xffffu, bytes, 0x00020000u};
}
DI void set_m0(unsigned lds) { asm volatile("s_mov_b32 m0, %0\n\ts_nop 0" :: "s"(lds) : "memory"); }
template <int OFF> DI void dma(u4 srd, unsigned soff, unsigned voff) {
    asm volatile("buffer_load_dwordx4 %2, %0, %1 offen offset:%3 lds" :: "s"(srd), "s"(soff), "v"(voff), "i"(OFF) : "memory");
}
template <int PH> DI void dma_piece(unsigned lds, u4 srd, unsigned soff, unsigned voff) {
    if (PH == 0) set_m0(lds);
    if (PH == 4) set_m0(lds + 4096);
    dma<(PH & 3) * 1024>(srd, soff, voff);
}
DI void touch(unsigned lds, u4 srd, unsigned voff) {
    asm volatile("s_mov_b32 m0, %0\n\ts_nop 0\n\tbuffer_load_dword %2, %1, 0 offen lds" :: "s"(lds), "s"(srd), "v"(voff) : "memory");
}
template <class F, int... I> DI void static_for_impl(F&& f, std::integer_sequence<int, I...>) { (f(std::integral_constant<int, I>{}), ...); }
template <int N, class F> DI void static_for(F&& f) { static_for_impl(f, std::make_integer_sequence<int, N>{}); }
#define SB() __builtin_amdgcn_sched_barrier(0)
#define WAIT_VM(n) asm volatile("s_waitcnt vmcnt(" #n ")" ::: "memory")
#define WAIT_VM_BAR(n) asm volatile("s_waitcnt vmcnt(" #n ") lgkmcnt(0)\n\ts_barrier" ::: "memory")
#define WAIT_LGKM() asm volatile("s_waitcnt lgkmcnt(0)" ::: "memory")
#define LGKM_BAR() asm volatile("s_waitcnt lgkmcnt(0)\n\ts_barrier" ::: "memory")

DI s4 tr_read(LAS char* p) { return __builtin_amdgcn_ds_read_tr16_b64_v4i16((LAS s4*)p); }
DI b8 tr_frag(LAS char* p0, LAS char* p1) {
    const s4 lo = tr_read(p0), hi = tr_read(p1);
    return __builtin_bit_cast(b8, __builtin_shufflevector(lo, hi, 0, 1, 2, 3, 4, 5, 6, 7));
}
DI unsigned pack2(float a, float b) { return __builtin_bit_cast(unsigned, __builtin_convertvector((f2){a, b}, b2)); }
DI b8 as_frag(unsigned a, unsigned b, unsigned c, unsigned d) { return __builtin_bit_cast(b8, (u4){a, b, c, d}); }
#define DEF_MFMA16(name, ca, cb)                                                                                          \
    template <class A, class B> DI void name(bool first, f4& acc, const A& a, const B& b) {                              \
        if (first) asm volatile("v_mfma_f32_16x16x32_bf16 %0, %1, %2, 0" : "=&v"(acc) : ca(a), cb(b));                    \
        else asm volatile("v_mfma_f32_16x16x32_bf16 %0, %1, %2, %0" : "+v"(acc) : ca(a), cb(b));                          \
    }
DEF_MFMA16(mfma16_vv, "v", "v")
DEF_MFMA16(mfma16_va, "v", "a")
DEF_MFMA16(mfma16_av, "a", "v")
DI void mfma32_pad(bool first, f16v& acc, const b8& a, const b8& b) {
    if (first) asm volatile("s_nop 1\n\tv_mfma_f32_32x32x16_bf16 %0, %1, %2, 0" : "=&v"(acc) : "v"(a), "v"(b));
    else asm volatile("s_nop 1\n\tv_mfma_f32_32x32x16_bf16 %0, %1, %2, %0" : "+v"(acc) : "v"(a), "v"(b));
}
DI void mfma32k8_pad(f16v& acc, const s4& a, const s4& b) {
    asm volatile("s_nop 1\n\tv_mfma_f32_32x32x8_bf16 %0, %1, %2, %0" : "+v"(acc) : "v"(a), "v"(b));
}
template <class T> DI void pin(T& a) { asm volatile("" : "+v"(a)); }
template <class T, class... R> DI void pin(T& a, R&... r) { pin(a); pin(r...); }
DI void landed(const b8& a, const b8& b) { asm volatile("" :: "a"(a), "a"(b) : "memory"); }
template <int N> DI void wait_vm() { asm volatile("s_waitcnt vmcnt(%0)" :: "i"(N) : "memory"); }

struct PhiW { b8 a0[2], a1[2]; s4 a2[2]; };

template <class F> DI void feature_map(const PhiW& w, const unsigned (&in)[10], unsigned (&out)[20], F&& hook) {
    const b8 b0 = as_frag(in[0], in[1], in[2], in[3]), b1 = as_frag(in[4], in[5], in[6], in[7]);
    const s4 b2_ = __builtin_bit_cast(s4, (u2){in[8], in[9]});
    f16v d0, d1;
    mfma32_pad(true, d0, w.a0[0], b0);
    mfma32_pad(false, d0, w.a1[0], b1);
    mfma32k8_pad(d0, w.a2[0], b2_);
    mfma32_pad(true, d1, w.a0[1], b0);
    mfma32_pad(false, d1, w.a1[1], b1);
    mfma32k8_pad(d1, w.a2[1], b2_);
    hook(std::integral_constant<int, 0>{});
    asm volatile("s_nop 7" : "+v"(d0));
    float p[20];
#pragma unroll
    for (int r = 0; r < 16; ++r) p[r] = d0[r];
    float mx = p[0], mn = p[0];
#pragma unroll
    for (int r = 1; r < 15; ++r) { mx = __builtin_elementwise_maximum(mx, p[r]); mn = __builtin_elementwise_minimum(mn, p[r]); }
    asm volatile("" : "+v"(mx), "+v"(mn), "+v"(d1));
#pragma unroll
    for (int r = 16; r < 20; ++r) p[r] = d1[r - 16];
#pragma unroll
    for (int r = 15; r < 20; ++r) { mx = __builtin_elementwise_maximum(mx, p[r]); mn = __builtin_elementwise_minimum(mn, p[r]); }
    constexpr float L = 1.4426950408889634f;
    const f2 cp = {-mx * L, -mx * L}, cn = {mn * L, mn * L};
    f2 e[10], f[10];
    static_for<10>([&](auto mc) {
        constexpr int m = mc;
        if (m == 0) { SB(); hook(std::integral_constant<int, 1>{}); SB(); }
        if (m == 4) { SB(); hook(std::integral_constant<int, 2>{}); SB(); }
        if (m == 8) { SB(); hook(std::integral_constant<int, 3>{}); SB(); }
        const f2 x = {p[2 * m], p[2 * m + 1]};
        const f2 a = __builtin_elementwise_fma(x, (f2){L, L}, cp), b = __builtin_elementwise_fma(x, (f2){-L, -L}, cn);
        e[m] = (f2){__builtin_amdgcn_exp2f(a[0]), __builtin_amdgcn_exp2f(a[1])};
        f[m] = (f2){__builtin_amdgcn_exp2f(b[0]), __builtin_amdgcn_exp2f(b[1])};
    });
    f2 se = (e[0] + e[1]) + (e[2] + e[3]) + ((e[4] + e[5]) + (e[6] + e[7])) + (e[8] + e[9]);
    f2 sf = (f[0] + f[1]) + (f[2] + f[3]) + ((f[4] + f[5]) + (f[6] + f[7])) + (f[8] + f[9]);
    const float re = __builtin_amdgcn_rcpf(se[0] + se[1]), rf = __builtin_amdgcn_rcpf(sf[0] + sf[1]);
    SB(); hook(std::integral_constant<int, 4>{}); SB();
#pragma unroll
    for (int m = 0; m < 10; ++m) {
        const f2 a = e[m] * (f2){re, re}, b = f[m] * (f2){rf, rf};
        out[m] = pack2(a[0], a[1]);
        out[10 + m] = pack2(b[0], b[1]);
    }
}

DI u64 load_flag(const GAS u64* p) { return __hip_atomic_load(p, __ATOMIC_RELAXED, __HIP_MEMORY_SCOPE_AGENT); }
DI void wait_flags(const GAS u64* p, u64 epoch, u64 v) {
#pragma unroll 1
    for (unsigned spins = 0; spins < SPIN_CAP && !__all(v == epoch); ++spins) v = load_flag(p);
    asm volatile("" ::: "memory");
}
DI void set_flag(GAS u64* p, u64 epoch) { __hip_atomic_store(p, epoch, __ATOMIC_RELAXED, __HIP_MEMORY_SCOPE_AGENT); }

#define STAMP(i) do { __builtin_amdgcn_sched_barrier(0); ts[i] = __builtin_amdgcn_s_memrealtime(); __builtin_amdgcn_sched_barrier(0); } while (0)

__global__ __launch_bounds__(256, 1) void hedgehog_fused(const char* __restrict__ x, const char* __restrict__ wqkv,
                                                         const char* __restrict__ wphi, const char* __restrict__ wo,
                                                         char* __restrict__ out, char* __restrict__ ws) {
    __shared__ __attribute__((aligned(16))) char lds_raw[L_SIZE];
    LAS char* const lds = (LAS char*)lds_raw;
    const unsigned lb = (unsigned)(size_t)lds;
    u64 ts[16];
    const u64 c0 = __builtin_amdgcn_s_memtime();
    STAMP(0);
    const unsigned b = blockIdx.x, xcd = b & 7, r = b >> 3;
    {
        u64 pc;
        asm volatile("s_getpc_b64 %0" : "=s"(pc));
        if (threadIdx.x < 8) touch(lb + L_JUNK, make_srd((const void*)(pc & ~127ull), KERNEL_BYTES), (r * 8 + threadIdx.x) * 128);
    }
    SB();
    const unsigned l = threadIdx.x & 63, w = __builtin_amdgcn_readfirstlane(threadIdx.x >> 6);
    const unsigned g = l >> 4, i = l & 15, h = l >> 5, c = l & 31, q = (l >> 2) & 3, p = l & 3, g1 = g & 1;
    const unsigned s = (xcd & 1) * 8 + (r & 7), hg = (xcd >> 1) * 4 + (r >> 3);
    const unsigned rt = xcd * 4 + (r & 3), ct = r >> 2;
    const unsigned rot = ((3 * s + hg) % 10) * 2;
    auto kstep = [&](unsigned it) { const unsigned k = rot + it; return k >= 20 ? k - 20 : k; };

    unsigned xo[4], wko[5];
#pragma unroll
    for (int m = 0; m < 4; ++m) {
        const unsigned row = 8 * m + (l >> 3);
        xo[m] = (s * 128 + 32 * w + row) * 1280 + (((l & 7) ^ ((row >> 1) & 7)) << 4) - 1024 * m;
    }
    asm volatile("" :: "s"(x), "s"(wqkv), "s"(wphi), "s"(wo), "s"(out), "s"(ws));
    STAMP(15);
    const u4 sx = make_srd(x, 2048 * 1280), sw = make_srd(wqkv, 640 * 3840), sp = make_srd(wphi, 32 * 800), so = make_srd(wo, 640 * 1280);
    const unsigned xr = L_XR + w * 12288;
    auto dma_x = [&](unsigned j, auto mc) { dma_piece<mc>(lb + xr + (j % 3) * 4096, sx, kstep(2 * j) * 64, xo[mc]); };
    static_for<4>([&](auto m) { dma_x(0, m); });
    static_for<4>([&](auto m) { dma_x(1, m); });
    SB();
#pragma unroll
    for (int ph = 0; ph < 5; ++ph) {
        const unsigned n = 64 * ph + l, u = n / 5, ch = n - 5 * u;
        const unsigned row = 16 * (u >> 5) + 8 * ((u >> 2) & 1) + 4 * ((u >> 4) & 1) + (u & 3);
        wko[ph] = row * 3840 + (1 + ((u >> 3) & 1)) * 1280 + hg * 80 + ch * 16 - (ph < 4 ? 1024 * ph : 0);
    }
    auto dma_wkv = [&](unsigned cc, auto ph) { dma_piece<ph>(lb + L_WKV + (4 * cc + w) * 5120, sw, kstep(4 * cc + w) * 122880, wko[ph]); };
    static_for<5>([&](auto ph) { dma_wkv(0, ph); });
    if (w < 2) dma_piece<0>(lb + L_WPHI + w * 1024, sp, hg * 1600 + w * 1024, l * 16);
    else if (w == 2) touch(lb + L_EPOCH, make_srd(ws, WS_END), l < 2 ? WS_EPOCH + 4 * l : (l & 3) << 20);
    else touch(lb + L_JUNK, make_srd(out, 2048 * 1280), rt * 81920 + (l & 1) * 81916);
    static_for<5>([&](auto ph) { dma_wkv(1, ph); });
    static_for<4>([&](auto m) { dma_x(2, m); });
    SB();
    if (l < 16) *(LAS unsigned*)(lds + L_ZERO + l * 4) = 0;

    unsigned kvt_dst[2];
#pragma unroll
    for (int pr = 0; pr < 2; ++pr) {
        const unsigned bi = 2 * pr + h, blk = (bi == 3 && w == 3) ? 14 : w + 4 * bi;
        const unsigned ci = 16 * blk + i, hh = ci >= 120, cc = ci - 120 * hh;
        const bool t1 = cc >= 80;
        const unsigned m = t1 ? cc - 80 : cc, sh = t1 ? 3 : 4, nfull = t1 ? 32 : 64;
        const bool f2_ = m >= nfull;
        const unsigned idx = f2_ ? 2 * (m - nfull) + g1 : m;
        const unsigned d = (t1 ? 16 : 0) + (idx & ((1u << sh) - 1)), f0 = (f2_ ? 32 : 16 * g1) + 4 * (idx >> sh);
        kvt_dst[pr] = L_KVT + hh * 1920 + d * 80 + f0 * 2;
    }
    unsigned wqo[5], woo[5];
#pragma unroll
    for (int m = 0; m < 5; ++m) {
        const unsigned ph = (w + 4 * m) % 5;
        const unsigned n = 64 * ph + l, u = n / 5, ch = n - 5 * u;
        const unsigned row = 32 * (u >> 5) + 16 * ((u >> 4) & 1) + 8 * ((u >> 3) & 1) + 4 * (u & 1) + ((u >> 1) & 3);
        wqo[m] = row * 3840 + hg * 80 + ch * 16;
    }
#pragma unroll
    for (int ph = 0; ph < 5; ++ph) {
        const unsigned n = 64 * ph + l, u = n / 10, ch = n - 10 * u;
        const unsigned row = 16 * (u >> 4) + 8 * ((u >> 2) & 1) + 4 * ((u >> 3) & 1) + (u & 3);
        woo[ph] = row * 1280 + ct * 160 + ch * 16 - (ph < 4 ? 1024 * ph : 0);
    }
    unsigned k0a, k1a, k0s, k1s;
    {
        const unsigned a_ = c >> 3, hv = (c >> 2) & 1, bb = c & 3;
        const unsigned d0 = 4 * h + 4 * a_ + bb, d1 = a_ ? 20 + bb : (h ? bb : 16 + bb);
        const bool v0 = hv == h, v1 = hv == h && a_ < 2;
        k0a = v0 ? L_KVT + h * 1920 + d0 * 80 : L_ZERO; k0s = v0 ? 16 : 0;
        k1a = v1 ? L_KVT + h * 1920 + d1 * 80 : L_ZERO; k1s = v1 ? 16 : 0;
    }
    asm volatile("" : "+v"(kvt_dst[0]), "+v"(kvt_dst[1]), "+v"(k0a), "+v"(k1a), "+v"(k0s), "+v"(k1s));
    asm volatile("" : "+v"(wqo[0]), "+v"(wqo[1]), "+v"(wqo[2]), "+v"(wqo[3]), "+v"(wqo[4]));
    asm volatile("" : "+v"(woo[0]), "+v"(woo[1]), "+v"(woo[2]), "+v"(woo[3]), "+v"(woo[4]));
    auto dma_wq = [&](auto nc) {
        const unsigned m = 4 * nc + w, pr = m / 5, ph = m - 5 * pr, pair = pr > 9 ? 9 : pr;
        dma_piece<0>(lb + L_WQ + pair * 5120 + ph * 1024, sw, kstep(2 * pair) * 122880, wqo[nc % 5]);
    };
    auto dma_wo = [&](auto nc) { const unsigned s2 = 4 * (nc / 5) + w; dma_piece<nc % 5>(lb + L_WO + s2 * 5120, so, s2 * 40960, woo[nc % 5]); };

    b8 xf[20][2];
    f4 acc[2][5];
    LAS char* const xrd0 = lds + xr + i * 128 + ((g ^ (i >> 1)) << 4);
    LAS char* const xrd1 = lds + xr + i * 128 + (((4 + g) ^ (i >> 1)) << 4);
    LAS char* const wrd = lds + L_WKV + (h * 32 + (p & 1) * 8 + g1 * 4 + q) * 80 + (p >> 1) * 40;
    auto w_frag = [&](int it, int t) { return tr_frag(wrd + it * 5120 + 8 * t, wrd + it * 5120 + 1280 + 8 * t); };
    PhiW pwk, pwq;
    u64 epoch = 0;
    b8 wa[5], wb[5];
    static_for<10>([&](auto jc) {
        constexpr int j = jc;
        if (j == 0) WAIT_VM_BAR(9); else if (j == 8) WAIT_VM_BAR(8); else if (j == 9) WAIT_VM(8); else if (j == 1) { } else if (j & 1) WAIT_VM(13); else WAIT_VM_BAR(13);
        if (j == 0) STAMP(1);
#pragma unroll
        for (int t = 0; t < 2; ++t) xf[2 * j][t] = *(LAS b8*)(xrd0 + (j % 3) * 4096 + t * 2048);
        if ((j & 1) == 0) {
#pragma unroll
            for (int t = 0; t < 5; ++t) wa[t] = w_frag(2 * j, t);
        }
#pragma unroll
        for (int t = 0; t < 2; ++t) xf[2 * j + 1][t] = *(LAS b8*)(xrd1 + (j % 3) * 4096 + t * 2048);
        SB();
        static_for<5>([&](auto t) {
            wb[t] = w_frag(2 * j + 1, t);
            mfma16_va(j == 0, acc[0][t], wa[t], xf[2 * j][0]);
            mfma16_va(j == 0, acc[1][t], wa[t], xf[2 * j][1]);
            SB();
        });
        landed(xf[2 * j + 1][0], xf[2 * j + 1][1]);
        auto refill = [&](auto nc) {
            constexpr int n = nc;
            if constexpr (j < 7) {
                if constexpr (j & 1) { if constexpr (n < 5) dma_wkv((j + 3) / 2, nc); else if constexpr (n < 9) dma_x(j + 3, std::integral_constant<int, (n < 9 ? n - 5 : 0)>{}); }
                else if constexpr (n < 4) dma_x(j + 3, nc);
            } else if constexpr (j < 9 && n < 4) dma_wq(std::integral_constant<int, 4 * (j - 7) + n>{});
        };
        static_for<5>([&](auto t) {
            if ((j & 1) == 0) wa[t] = w_frag(2 * j + 2, t);
            mfma16_va(false, acc[0][t], wb[t], xf[2 * j + 1][0]);
            refill(std::integral_constant<int, 2 * t>{});
            mfma16_va(false, acc[1][t], wb[t], xf[2 * j + 1][1]);
            refill(std::integral_constant<int, 2 * t + 1>{});
            SB();
        });
        if (j == 1) {
            const u2 ew = *(LAS u2*)(lds + L_EPOCH);
            epoch = (((u64)(unsigned)__builtin_amdgcn_readfirstlane(ew[1]) << 32) | (unsigned)__builtin_amdgcn_readfirstlane(ew[0])) * 6364136223846793005ull + 1442695040888963407ull;
            const unsigned a_ = 2 * g1 + (p >> 1);
#pragma unroll
            for (int t = 0; t < 2; ++t) {
                const bool valid = (p & 1) == h && (t == 0 || a_ == 0);
                const unsigned base = L_WPHI + h * 800 + q * 40 + (16 * t + 4 * a_) * 2;
                s4 fk[5], fq[5];
#pragma unroll
                for (int m = 0; m < 5; ++m) fk[m] = tr_read(lds + (valid ? base + m * 160 : L_ZERO));
#pragma unroll
                for (int m = 0; m < 5; ++m) fq[m] = tr_read(lds + (valid ? base + (h ? (m + 1) % 5 : m) * 160 : L_ZERO));
                pwk.a0[t] = __builtin_bit_cast(b8, __builtin_shufflevector(fk[0], fk[1], 0, 1, 2, 3, 4, 5, 6, 7));
                pwk.a1[t] = __builtin_bit_cast(b8, __builtin_shufflevector(fk[2], fk[3], 0, 1, 2, 3, 4, 5, 6, 7));
                pwk.a2[t] = fk[4];
                pwq.a0[t] = __builtin_bit_cast(b8, __builtin_shufflevector(fq[0], fq[1], 0, 1, 2, 3, 4, 5, 6, 7));
                pwq.a1[t] = __builtin_bit_cast(b8, __builtin_shufflevector(fq[2], fq[3], 0, 1, 2, 3, 4, 5, 6, 7));
                pwq.a2[t] = fq[4];
            }
            SB();
        }
    });
    STAMP(2);

    {
        unsigned kk[10], vv[10], ph[20];
        asm volatile("" : "+v"(acc[0][0]), "+v"(acc[1][0]), "+v"(acc[0][1]), "+v"(acc[1][1]), "+v"(acc[0][2]), "+v"(acc[1][2]));
        static_for<10>([&](auto mc) {
            constexpr int m = mc;
            if (m == 6) { pin(kk[0], kk[1], kk[2], kk[3], kk[4], kk[5]); pin(acc[0][3], acc[1][3], acc[0][4], acc[1][4]); }
            const unsigned a0 = pack2(acc[0][m >> 1][2 * (m & 1)], acc[0][m >> 1][2 * (m & 1) + 1]);
            const unsigned a1 = pack2(acc[1][m >> 1][2 * (m & 1)], acc[1][m >> 1][2 * (m & 1) + 1]);
            const auto sw2 = __builtin_amdgcn_permlane16_swap(a0, a1, false, false);
            kk[m] = sw2[0]; vv[m] = sw2[1];
        });
        LAS u4* const vrow = (LAS u4*)(lds + xr + 5120 + l * 48);
        feature_map(pwk, kk, ph, [&](auto n) {
            if (n == 0) {
                vrow[0] = (u4){vv[0], vv[1], vv[2], vv[3]};
                vrow[1] = (u4){vv[4], vv[5], vv[6], vv[7]};
                vrow[2] = (u4){vv[8], vv[9], 0x00003f80u, 0u};
            }
            dma_wq(std::integral_constant<int, 8 + n>{});
        });
        LAS u4* const prow = (LAS u4*)(lds + xr + l * 80);
#pragma unroll
        for (int m = 0; m < 5; ++m) prow[m] = (u4){ph[4 * m], ph[4 * m + 1], ph[4 * m + 2], ph[4 * m + 3]};
    }
    LGKM_BAR();
    STAMP(3);
    const __amdgpu_buffer_rsrc_t rws = __builtin_amdgcn_make_buffer_rsrc(ws, 0, WS_END, 0x00020000);
    {
        const unsigned hh = w >> 1, tt = w & 1, rho = hh * 32 + 16 * h + 2 * (4 * g1 + q);
        LAS char* const pa = lds + L_XR + rho * 80 + 8 * p;
        LAS char* const pb = lds + L_XR + 5120 + rho * 48 + 32 * tt + 8 * p;
        b8 fa[4][3], fb[4];
#pragma unroll
        for (int st = 0; st < 4; ++st) {
            fb[st] = tr_frag(pb + st * 12288, pb + st * 12288 + 48);
#pragma unroll
            for (int f = 0; f < 3; ++f) fa[st][f] = tr_frag(pa + st * 12288 + 32 * f, pa + st * 12288 + 80 + 32 * f);
        }
        SB();
        f4 kv[3];
        static_for<4>([&](auto st) { static_for<3>([&](auto f) { mfma16_vv(st == 0, kv[f], fa[st][f], fb[st]); }); });
        const unsigned rec = WS_KVP + (hg * 16 + s) * 3840 + hh * 1920 + tt * 1280;
        const unsigned slot = tt ? 8 * g + i : l;
        const bool on = tt == 0 || i < 8;
        const unsigned big = 0x7ffffff0u;
        unsigned o0 = on ? slot * 16 : big, o1 = (on && g < 2) ? (tt ? 512 : 1024) + slot * 8 : big;
        asm volatile("s_nop 7" : "+v"(kv[0]), "+v"(kv[1]), "+v"(kv[2]), "+v"(o0), "+v"(o1));
        __builtin_amdgcn_raw_buffer_store_b128((u4){pack2(kv[0][0], kv[0][1]), pack2(kv[0][2], kv[0][3]), pack2(kv[1][0], kv[1][1]), pack2(kv[1][2], kv[1][3])}, rws, o0, rec, 16);
        __builtin_amdgcn_raw_buffer_store_b64((u2){pack2(kv[2][0], kv[2][1]), pack2(kv[2][2], kv[2][3])}, rws, o1, rec, 16);
    }
    WAIT_VM_BAR(2);
    STAMP(4);

    f4 aq[2][3];
    const GAS u64* const fl1 = (const GAS u64*)(ws + WS_FL1 + (hg * 16 + (l >> 2)) * 128 + (l & 3) * 8);
    {
        LAS char* const qb = lds + L_WQ + (16 * h + 2 * (4 * g1 + q)) * 80;
        LAS char* const q01 = qb + (p < 2 ? 8 * p : 32 + 8 * p);
        LAS char* const q2 = qb + ((p == 1 || p == 2) ? 40 : 32);
        auto q_frag = [&](int it, int t) { LAS char* const a = (t < 2 ? q01 + 16 * t : q2) + it * 2560; return tr_frag(a, a + 80); };
        b8 qf[3][3];
#pragma unroll
        for (int it = 0; it < 2; ++it)
#pragma unroll
            for (int t = 0; t < 3; ++t) qf[it][t] = q_frag(it, t);
        SB();
        static_for<20>([&](auto itc) {
            constexpr int it = itc;
            if (it == FLAG1_IT) {
                wait_vm<(FLAG1_IT < WO_IN_Q ? FLAG1_IT : WO_IN_Q)>();
                if (l == 0) set_flag((GAS u64*)(ws + WS_FL1 + (hg * 16 + s) * 128 + w * 8), epoch);
                SB();
            }
            static_for<3>([&](auto t) {
                if (it + 2 < 20) qf[(it + 2) % 3][t] = q_frag(it + 2, t);
                mfma16_va(it == 0, aq[0][t], qf[it % 3][t], xf[it][0]);
                if constexpr (t == 0 && it < WO_IN_Q) dma_wo(itc);
                mfma16_va(it == 0, aq[1][t], qf[it % 3][t], xf[it][1]);
                SB();
            });
        });
    }
    STAMP(5);
    u64 fv = load_flag(fl1);
    SB();
    u4 part[4][4];
    unsigned phq[20];
    {
        unsigned qq[10];
        asm volatile("s_nop 6" : "+v"(aq[0][0]), "+v"(aq[1][0]), "+v"(aq[0][1]), "+v"(aq[1][1]), "+v"(aq[0][2]), "+v"(aq[1][2]));
#pragma unroll
        for (int t = 0; t < 3; ++t)
#pragma unroll
            for (int e = 0; e < 2; ++e) {
                const auto sw2 = __builtin_amdgcn_permlane16_swap(pack2(aq[0][t][2 * e], aq[0][t][2 * e + 1]), pack2(aq[1][t][2 * e], aq[1][t][2 * e + 1]), false, false);
                qq[4 * t + e] = sw2[0];
                if (t < 2) qq[4 * t + 2 + e] = sw2[1];
            }
        feature_map(pwq, qq, phq, [&](auto n) {
            if constexpr (n == GATHER_HOOK) {
                wait_flags(fl1, epoch, fv);
                STAMP(6);
#pragma unroll
                for (int bi = 0; bi < 4; ++bi) {
                    const unsigned blk = (bi == 3 && w == 3) ? 14 : w + 4 * bi;
#pragma unroll
                    for (int st = 0; st < 4; ++st)
                        part[bi][st] = __builtin_amdgcn_raw_buffer_load_b128(rws, g * 3840 + i * 16, WS_KVP + (hg * 16 + 4 * st) * 3840 + blk * 256, 16);
                }
            }
            if constexpr (n == 4) static_for<25 - WO_IN_Q>([&](auto m) { dma_wo(std::integral_constant<int, WO_IN_Q + m>{}); });
        });
#pragma unroll
        for (int m = 0; m < 20; m += 4) asm volatile("" : "+v"(phq[m]), "+v"(phq[m + 1]), "+v"(phq[m + 2]), "+v"(phq[m + 3]));
    }
    SB();
    STAMP(7);
    {
        b8 sel[2];
#pragma unroll
        for (int e = 0; e < 2; ++e) {
            u4 v;
#pragma unroll
            for (int m = 0; m < 4; ++m) v[m] = i == 8 * e + 2 * m ? 0x00003f80u : i == 8 * e + 2 * m + 1 ? 0x3f800000u : 0u;
            sel[e] = __builtin_bit_cast(b8, v);
        }
        asm volatile("s_nop 1" : "+v"(sel[0]), "+v"(sel[1]));
        f4 sum[2];
        static_for<2>([&](auto pr) { static_for<8>([&](auto n) { mfma16_vv(n == 0, sum[pr], sel[n / 4], part[2 * pr + n / 4][n % 4]); }); });
        asm volatile("" : "+v"(sum[0]));
        *(LAS u2*)(lds + kvt_dst[0]) = (u2){pack2(sum[0][0], sum[0][1]), pack2(sum[0][2], sum[0][3])};
        asm volatile("s_nop 4" : "+v"(sum[1]) :: "memory");
        *(LAS u2*)(lds + kvt_dst[1]) = (u2){pack2(sum[1][0], sum[1][1]), pack2(sum[1][2], sum[1][3])};
    }
    WAIT_VM_BAR(0);
    STAMP(8);
    {
        b8 ka[5], kb[5];
#pragma unroll
        for (int st = 0; st < 5; ++st) { kb[st] = *(LAS b8*)(lds + k1a + st * k1s); ka[st] = *(LAS b8*)(lds + k0a + st * k0s); }
        SB();
        f16v n0, n1;
        static_for<5>([&](auto st) {
            const b8 bq = as_frag(phq[4 * st], phq[4 * st + 1], phq[4 * st + 2], phq[4 * st + 3]);
            mfma32_pad(st == 0, n1, kb[st], bq);
        });
        static_for<5>([&](auto st) {
            const b8 bq = as_frag(phq[4 * st], phq[4 * st + 1], phq[4 * st + 2], phq[4 * st + 3]);
            mfma32_pad(st == 0, n0, ka[st], bq);
        });
        asm volatile("" : "+v"(n1));
        float inv = __builtin_amdgcn_rcpf(n1[4] + 1e-6f);
        unsigned o[10];
        o[8] = pack2(n1[0] * inv, n1[1] * inv);
        o[9] = pack2(n1[2] * inv, n1[3] * inv);
        const unsigned ob = WS_O + (s * 8 + 2 * w) * 20480 + hg * 1280;
        const unsigned vo = (c >> 4) * 20480 + i * 16;
        __builtin_amdgcn_raw_buffer_store_b64((u2){o[8], o[9]}, rws, vo + 512 + h * 8, ob, 16);
        asm volatile("s_nop 3" : "+v"(n0), "+v"(inv) :: "memory");
#pragma unroll
        for (int m = 0; m < 8; ++m) o[m] = pack2(n0[2 * m] * inv, n0[2 * m + 1] * inv);
        __builtin_amdgcn_raw_buffer_store_b128((u4){o[0], o[1], o[2], o[3]}, rws, vo + h * 768, ob, 16);
        __builtin_amdgcn_raw_buffer_store_b128((u4){o[4], o[5], o[6], o[7]}, rws, vo + h * 768 + 256, ob, 16);
        SB();
        asm volatile("s_nop 1" ::: "memory");
        SB();
    }
    STAMP(9);

    LAS char* const word = lds + L_WO + (16 * h + 4 * g1 + q) * 160 + 8 * p;
    auto o_frag = [&](int s2, int t) { return tr_frag(word + s2 * 5120 + 32 * t, word + s2 * 5120 + 1280 + 32 * t); };
    b8 wf[20][5];
    static_for<16>([&](auto s2) {
        if (s2 == PRE_BEFORE_FLAG) {
            WAIT_VM(0);
            if (l == 0) set_flag((GAS u64*)(ws + WS_FL2 + ((s * 4 + w) * 16 + hg) * 128), epoch);
            STAMP(10);
        }
#pragma unroll
        for (int t = 0; t < 5; ++t) wf[s2][t] = o_frag(s2, t);
        SB();
    });
    const GAS u64* const fl2 = (const GAS u64*)(ws + WS_FL2 + ((rt * 2 + (w >> 1)) * 16 + i) * 128);
    wait_flags(fl2, epoch, load_flag(fl2));
    STAMP(11);
    u4 of[20];
#pragma unroll
    for (int s2 = 0; s2 < 20; ++s2) of[s2] = __builtin_amdgcn_raw_buffer_load_b128(rws, l * 16, WS_O + (rt * 4 + w) * 20480 + s2 * 1024, 16);
    SB();
    f4 ao[5];
    static_for<20>([&](auto s2c) {
        constexpr int s2 = s2c;
        static_for<5>([&](auto t) {
            if constexpr (s2 >= 13 && s2 < 17) wf[s2 + 3][t] = o_frag(s2 + 3, t);
            if constexpr (s2 < 12) mfma16_av(s2 == 0, ao[t], wf[s2][t], of[s2]);
            else mfma16_vv(false, ao[t], wf[s2][t], of[s2]);
            SB();
        });
    });
    STAMP(12);
    {
        GAS char* const orow = (GAS char*)out + (rt * 64 + 16 * w + i) * 1280 + ct * 160;
        asm volatile("s_nop 3" : "+v"(ao[0]), "+v"(ao[1]));
        static_for<2>([&](auto tc) {
            constexpr int t = 2 * tc;
            if (t == 2) { SB(); pin(ao[2], ao[3], ao[4]); }
            const auto lo = __builtin_amdgcn_permlane16_swap(pack2(ao[t][0], ao[t][1]), pack2(ao[t + 1][0], ao[t + 1][1]), false, false);
            const auto hi = __builtin_amdgcn_permlane16_swap(pack2(ao[t][2], ao[t][3]), pack2(ao[t + 1][2], ao[t + 1][3]), false, false);
            *(GAS u4*)(orow + 32 * t + 32 * g1 + 16 * h) = (u4){lo[0], hi[0], lo[1], hi[1]};
        });
        *(GAS u2*)(orow + 128 + 8 * g) = (u2){pack2(ao[4][0], ao[4][1]), pack2(ao[4][2], ao[4][3])};
    }
    if (b == 0 && threadIdx.x == 0) set_flag((GAS u64*)(ws + WS_EPOCH), epoch);
    STAMP(13);
    WAIT_VM(0);
    STAMP(14);
    const u64 c1 = __builtin_amdgcn_s_memtime();
    {
        const u64 span = PROBE_K == 99 ? (c1 - c0) / 24 : ts[PROBE_K == 99 ? 0 : PROBE_K] - ts[PROBE_K0];
        const u64 until = ts[14] + span * 20;
        while (__builtin_amdgcn_s_memrealtime() < until) __builtin_amdgcn_s_sleep(8);
    }
    ts[13] = ts[0] + (c1 - c0) / 24;
    if (l == 0) {
        GAS u64* dg = (GAS u64*)(ws + WS_DIAG) + (b * 4 + w) * 16;
#pragma unroll
        for (int k = 0; k < 16; ++k) dg[k] = ts[k];
    }
}

extern "C" void kernel_launch(void* const* d_in, const int* in_sizes, int n_in, void* d_out, int out_size, void* d_ws,
                              size_t ws_size, hipStream_t stream) {
    hedgehog_fused<<<256, 256, 0, stream>>>((const char*)d_in[0], (const char*)d_in[1], (const char*)d_in[2],
                                            (const char*)d_in[3], (char*)d_out, (char*)d_ws);
}
```

```cpp
#define DIAG 1
#include <hip/hip_runtime.h>
#include <hip/hip_bf16.h>
#include <utility>

#define DI __device__ __forceinline__
#define GAS __attribute__((address_space(1)))
#define LAS __attribute__((address_space(3)))
#define CAS __attribute__((address_space(4)))
typedef unsigned long long u64;
typedef unsigned u2 __attribute__((ext_vector_type(2)));
typedef unsigned u4 __attribute__((ext_vector_type(4)));
typedef short s4 __attribute__((ext_vector_type(4)));
typedef short s8 __attribute__((ext_vector_type(8)));
typedef __bf16 b2 __attribute__((ext_vector_type(2)));
typedef __bf16 b8 __attribute__((ext_vector_type(8)));
typedef float f2 __attribute__((ext_vector_type(2)));
typedef float f4 __attribute__((ext_vector_type(4)));
typedef float f16v __attribute__((ext_vector_type(16)));

constexpr unsigned WS_EPOCH = 0;
constexpr unsigned WS_FL1 = 4096;
constexpr unsigned WS_FL2 = 65536;
constexpr unsigned WS_KVP = 262144;
constexpr unsigned WS_O = 1310720;
constexpr unsigned WS_END = WS_O + 2048 * 640 * 2;
constexpr unsigned WS_DIAG = 8u << 20;

constexpr unsigned L_WKV = 0;
constexpr unsigned L_WQ = 0;
constexpr unsigned L_WO = 51200;
constexpr unsigned L_XR = 102400;
constexpr unsigned L_WPHI = 153600;
constexpr unsigned L_ZERO = 155648;
constexpr unsigned L_JUNK = 155904;
constexpr unsigned L_EPOCH = 156160;
constexpr unsigned L_KVT = 156416;
constexpr unsigned L_SIZE = 163840;

constexpr unsigned SPIN_CAP = 1u << 21;

DI u4 make_srd(const void* p, unsigned bytes) {
    const u64 a = (u64)p;
    return (u4){(unsigned)a, (unsigned)(a >> 32) & 0xffffu, bytes, 0x00020000u};
}
DI void set_m0(unsigned lds) { asm volatile("s_mov_b32 m0, %0\n\ts_nop 0" :: "s"(lds) : "memory"); }
template <int OFF> DI void dma(u4 srd, unsigned soff, unsigned voff) {
    asm volatile("buffer_load_dwordx4 %2, %0, %1 offen offset:%3 lds" :: "s"(srd), "s"(soff), "v"(voff), "i"(OFF) : "memory");
}
template <int PH> DI void dma_piece(unsigned lds, u4 srd, unsigned soff, unsigned voff) {
    if (PH == 0) set_m0(lds);
    if (PH == 4) set_m0(lds + 4096);
    dma<(PH & 3) * 1024>(srd, soff, voff);
}
DI void touch(unsigned lds, u4 srd, unsigned voff) {
    asm volatile("s_mov_b32 m0, %0\n\ts_nop 0\n\tbuffer_load_dword %2, %1, 0 offen lds" :: "s"(lds), "s"(srd), "v"(voff) : "memory");
}
template <class F, int... I> DI void static_for_impl(F&& f, std::integer_sequence<int, I...>) { (f(std::integral_constant<int, I>{}), ...); }
template <int N, class F> DI void static_for(F&& f) { static_for_impl(f, std::make_integer_sequence<int, N>{}); }
#define SB() __builtin_amdgcn_sched_barrier(0)
#define WAIT_VM(n) asm volatile("s_waitcnt vmcnt(" #n ")" ::: "memory")
#define WAIT_VM_BAR(n) asm volatile("s_waitcnt vmcnt(" #n ") lgkmcnt(0)\n\ts_barrier" ::: "memory")
#define WAIT_LGKM() asm volatile("s_waitcnt lgkmcnt(0)" ::: "memory")
#define LGKM_BAR() asm volatile("s_waitcnt lgkmcnt(0)\n\ts_barrier" ::: "memory")

DI s4 tr_read(LAS char* p) { return __builtin_amdgcn_ds_read_tr16_b64_v4i16((LAS s4*)p); }
DI b8 tr_frag(LAS char* p0, LAS char* p1) {
    const s4 lo = tr_read(p0), hi = tr_read(p1);
    return __builtin_bit_cast(b8, __builtin_shufflevector(lo, hi, 0, 1, 2, 3, 4, 5, 6, 7));
}
DI unsigned pack2(float a, float b) { return __builtin_bit_cast(unsigned, __builtin_convertvector((f2){a, b}, b2)); }
DI b8 as_frag(unsigned a, unsigned b, unsigned c, unsigned d) { return __builtin_bit_cast(b8, (u4){a, b, c, d}); }
#define DEF_MFMA16(name, ca, cb)                                                                                          \
    template <class A, class B> DI void name(bool first, f4& acc, const A& a, const B& b) {                              \
        if (first) asm volatile("v_mfma_f32_16x16x32_bf16 %0, %1, %2, 0" : "=&v"(acc) : ca(a), cb(b));                    \
        else asm volatile("v_mfma_f32_16x16x32_bf16 %0, %1, %2, %0" : "+v"(acc) : ca(a), cb(b));                          \
    }
DEF_MFMA16(mfma16_vv, "v", "v")
DEF_MFMA16(mfma16_va, "v", "a")
DEF_MFMA16(mfma16_av, "a", "v")
DI void mfma32_pad(bool first, f16v& acc, const b8& a, const b8& b) {
    if (first) asm volatile("s_nop 1\n\tv_mfma_f32_32x32x16_bf16 %0, %1, %2, 0" : "=&v"(acc) : "v"(a), "v"(b));
    else asm volatile("s_nop 1\n\tv_mfma_f32_32x32x16_bf16 %0, %1, %2, %0" : "+v"(acc) : "v"(a), "v"(b));
}
DI void mfma32k8_pad(f16v& acc, const s4& a, const s4& b) {
    asm volatile("s_nop 1\n\tv_mfma_f32_32x32x8_bf16 %0, %1, %2, %0" : "+v"(acc) : "v"(a), "v"(b));
}
template <class T> DI void pin(T& a) { asm volatile("" : "+v"(a)); }
template <class T, class... R> DI void pin(T& a, R&... r) { pin(a); pin(r...); }
DI void landed(const b8& a, const b8& b) { asm volatile("" :: "a"(a), "a"(b) : "memory"); }
template <int N> DI void wait_vm() { asm volatile("s_waitcnt vmcnt(%0)" :: "i"(N) : "memory"); }

struct PhiW { b8 a0[2], a1[2]; s4 a2[2]; };

template <class F> DI void feature_map(const PhiW& w, const unsigned (&in)[10], unsigned (&out)[20], F&& hook) {
    const b8 b0 = as_frag(in[0], in[1], in[2], in[3]), b1 = as_frag(in[4], in[5], in[6], in[7]);
    const s4 b2_ = __builtin_bit_cast(s4, (u2){in[8], in[9]});
    f16v d0, d1;
    mfma32_pad(true, d0, w.a0[0], b0);
    mfma32_pad(false, d0, w.a1[0], b1);
    mfma32k8_pad(d0, w.a2[0], b2_);
    mfma32_pad(true, d1, w.a0[1], b0);
    mfma32_pad(false, d1, w.a1[1], b1);
    mfma32k8_pad(d1, w.a2[1], b2_);
    hook(std::integral_constant<int, 0>{});
    asm volatile("s_nop 7" : "+v"(d0));
    float p[20];
#pragma unroll
    for (int r = 0; r < 16; ++r) p[r] = d0[r];
    float mx = p[0], mn = p[0];
#pragma unroll
    for (int r = 1; r < 15; ++r) { mx = __builtin_elementwise_maximum(mx, p[r]); mn = __builtin_elementwise_minimum(mn, p[r]); }
    asm volatile("" : "+v"(mx), "+v"(mn), "+v"(d1));
#pragma unroll
    for (int r = 16; r < 20; ++r) p[r] = d1[r - 16];
#pragma unroll
    for (int r = 15; r < 20; ++r) { mx = __builtin_elementwise_maximum(mx, p[r]); mn = __builtin_elementwise_minimum(mn, p[r]); }
    constexpr float L = 1.4426950408889634f;
    const f2 cp = {-mx * L, -mx * L}, cn = {mn * L, mn * L};
    f2 e[10], f[10];
    static_for<10>([&](auto mc) {
        constexpr int m = mc;
        if (m == 0) { SB(); hook(std::integral_constant<int, 1>{}); SB(); }
        if (m == 4) { SB(); hook(std::integral_constant<int, 2>{}); SB(); }
        if (m == 8) { SB(); hook(std::integral_constant<int, 3>{}); SB(); }
        const f2 x = {p[2 * m], p[2 * m + 1]};
        const f2 a = __builtin_elementwise_fma(x, (f2){L, L}, cp), b = __builtin_elementwise_fma(x, (f2){-L, -L}, cn);
        e[m] = (f2){__builtin_amdgcn_exp2f(a[0]), __builtin_amdgcn_exp2f(a[1])};
        f[m] = (f2){__builtin_amdgcn_exp2f(b[0]), __builtin_amdgcn_exp2f(b[1])};
    });
    f2 se = (e[0] + e[1]) + (e[2] + e[3]) + ((e[4] + e[5]) + (e[6] + e[7])) + (e[8] + e[9]);
    f2 sf = (f[0] + f[1]) + (f[2] + f[3]) + ((f[4] + f[5]) + (f[6] + f[7])) + (f[8] + f[9]);
    const float re = __builtin_amdgcn_rcpf(se[0] + se[1]), rf = __builtin_amdgcn_rcpf(sf[0] + sf[1]);
    SB(); hook(std::integral_constant<int, 4>{}); SB();
#pragma unroll
    for (int m = 0; m < 10; ++m) {
        const f2 a = e[m] * (f2){re, re}, b = f[m] * (f2){rf, rf};
        out[m] = pack2(a[0], a[1]);
        out[10 + m] = pack2(b[0], b[1]);
    }
}

DI u64 load_flag(const GAS u64* p) { return __hip_atomic_load(p, __ATOMIC_RELAXED, __HIP_MEMORY_SCOPE_AGENT); }
DI void wait_flags(const GAS u64* p, u64 epoch, u64 v) {
#pragma unroll 1
    for (unsigned spins = 0; spins < SPIN_CAP && !__all(v == epoch); ++spins) v = load_flag(p);
    asm volatile("" ::: "memory");
}
DI void set_flag(GAS u64* p, u64 epoch) { __hip_atomic_store(p, epoch, __ATOMIC_RELAXED, __HIP_MEMORY_SCOPE_AGENT); }

#define STAMP(i) do { __builtin_amdgcn_sched_barrier(0); ts[i] = __builtin_amdgcn_s_memrealtime(); __builtin_amdgcn_sched_barrier(0); } while (0)

__global__ __launch_bounds__(256, 1) void hedgehog_fused(const char* __restrict__ x, const char* __restrict__ wqkv,
                                                         const char* __restrict__ wphi, const char* __restrict__ wo,
                                                         char* __restrict__ out, char* __restrict__ ws) {
    __shared__ __attribute__((aligned(16))) char lds_raw[L_SIZE];
    LAS char* const lds = (LAS char*)lds_raw;
    const unsigned lb = (unsigned)(size_t)lds;
    u64 ts[16];
    const u64 c0 = __builtin_amdgcn_s_memtime();
    STAMP(0);
    const unsigned b = blockIdx.x, xcd = b & 7, r = b >> 3;
    {
        u64 pc;
        asm volatile("s_getpc_b64 %0" : "=s"(pc));
        if (threadIdx.x < 8) touch(lb + L_JUNK, make_srd((const void*)(pc & ~127ull), KERNEL_BYTES), (r * 8 + threadIdx.x) * 128);
    }
    SB();
    const unsigned l = threadIdx.x & 63, w = __builtin_amdgcn_readfirstlane(threadIdx.x >> 6);
    const unsigned g = l >> 4, i = l & 15, h = l >> 5, c = l & 31, q = (l >> 2) & 3, p = l & 3, g1 = g & 1;
    const unsigned s = (xcd & 1) * 8 + (r & 7), hg = (xcd >> 1) * 4 + (r >> 3);
    const unsigned rt = xcd * 4 + (r & 3), ct = r >> 2;
    const unsigned rot = ((3 * s + hg) % 10) * 2;
    auto kstep = [&](unsigned it) { const unsigned k = rot + it; return k >= 20 ? k - 20 : k; };

    unsigned xo[4], wko[5];
#pragma unroll
    for (int m = 0; m < 4; ++m) {
        const unsigned row = 8 * m + (l >> 3);
        xo[m] = (s * 128 + 32 * w + row) * 1280 + (((l & 7) ^ ((row >> 1) & 7)) << 4) - 1024 * m;
    }
    asm volatile("" :: "s"(x), "s"(wqkv), "s"(wphi), "s"(wo), "s"(out), "s"(ws));
    STAMP(15);
    const u4 sx = make_srd(x, 2048 * 1280), sw = make_srd(wqkv, 640 * 3840), sp = make_srd(wphi, 32 * 800), so = make_srd(wo, 640 * 1280);
    const unsigned xr = L_XR + w * 12288;
    auto dma_x = [&](unsigned j, auto mc) { dma_piece<mc>(lb + xr + (j % 3) * 4096, sx, kstep(2 * j) * 64, xo[mc]); };
    static_for<4>([&](auto m) { dma_x(0, m); });
    static_for<4>([&](auto m) { dma_x(1, m); });
    SB();
#pragma unroll
    for (int ph = 0; ph < 5; ++ph) {
        const unsigned n = 64 * ph + l, u = n / 5, ch = n - 5 * u;
        const unsigned row = 16 * (u >> 5) + 8 * ((u >> 2) & 1) + 4 * ((u >> 4) & 1) + (u & 3);
        wko[ph] = row * 3840 + (1 + ((u >> 3) & 1)) * 1280 + hg * 80 + ch * 16 - (ph < 4 ? 1024 * ph : 0);
    }
    auto dma_wkv = [&](unsigned cc, auto ph) { dma_piece<ph>(lb + L_WKV + (4 * cc + w) * 5120, sw, kstep(4 * cc + w) * 122880, wko[ph]); };
    static_for<5>([&](auto ph) { dma_wkv(0, ph); });
    if (w < 2) dma_piece<0>(lb + L_WPHI + w * 1024, sp, hg * 1600 + w * 1024, l * 16);
    else if (w == 2) touch(lb + L_EPOCH, make_srd(ws, WS_END), l < 2 ? WS_EPOCH + 4 * l : (l & 3) << 20);
    else touch(lb + L_JUNK, make_srd(out, 2048 * 1280), rt * 81920 + (l & 1) * 81916);
    static_for<5>([&](auto ph) { dma_wkv(1, ph); });
    static_for<4>([&](auto m) { dma_x(2, m); });
    SB();
    if (l < 16) *(LAS unsigned*)(lds + L_ZERO + l * 4) = 0;

    unsigned kvt_dst[2];
#pragma unroll
    for (int pr = 0; pr < 2; ++pr) {
        const unsigned bi = 2 * pr + h, blk = (bi == 3 && w == 3) ? 14 : w + 4 * bi;
        const unsigned ci = 16 * blk + i, hh = ci >= 120, cc = ci - 120 * hh;
        const bool t1 = cc >= 80;
        const unsigned m = t1 ? cc - 80 : cc, sh = t1 ? 3 : 4, nfull = t1 ? 32 : 64;
        const bool f2_ = m >= nfull;
        const unsigned idx = f2_ ? 2 * (m - nfull) + g1 : m;
        const unsigned d = (t1 ? 16 : 0) + (idx & ((1u << sh) - 1)), f0 = (f2_ ? 32 : 16 * g1) + 4 * (idx >> sh);
        kvt_dst[pr] = L_KVT + hh * 1920 + d * 80 + f0 * 2;
    }
    unsigned wqo[5], woo[5];
#pragma unroll
    for (int m = 0; m < 5; ++m) {
        const unsigned ph = (w + 4 * m) % 5;
        const unsigned n = 64 * ph + l, u = n / 5, ch = n - 5 * u;
        const unsigned row = 32 * (u >> 5) + 16 * ((u >> 4) & 1) + 8 * ((u >> 3) & 1) + 4 * (u & 1) + ((u >> 1) & 3);
        wqo[m] = row * 3840 + hg * 80 + ch * 16;
    }
#pragma unroll
    for (int ph = 0; ph < 5; ++ph) {
        const unsigned n = 64 * ph + l, u = n / 10, ch = n - 10 * u;
        const unsigned row = 16 * (u >> 4) + 8 * ((u >> 2) & 1) + 4 * ((u >> 3) & 1) + (u & 3);
        woo[ph] = row * 1280 + ct * 160 + ch * 16 - (ph < 4 ? 1024 * ph : 0);
    }
    unsigned k0a, k1a, k0s, k1s;
    {
        const unsigned a_ = c >> 3, hv = (c >> 2) & 1, bb = c & 3;
        const unsigned d0 = 4 * h + 4 * a_ + bb, d1 = a_ ? 20 + bb : (h ? bb : 16 + bb);
        const bool v0 = hv == h, v1 = hv == h && a_ < 2;
        k0a = v0 ? L_KVT + h * 1920 + d0 * 80 : L_ZERO; k0s = v0 ? 16 : 0;
        k1a = v1 ? L_KVT + h * 1920 + d1 * 80 : L_ZERO; k1s = v1 ? 16 : 0;
    }
    asm volatile("" : "+v"(kvt_dst[0]), "+v"(kvt_dst[1]), "+v"(k0a), "+v"(k1a), "+v"(k0s), "+v"(k1s));
    asm volatile("" : "+v"(wqo[0]), "+v"(wqo[1]), "+v"(wqo[2]), "+v"(wqo[3]), "+v"(wqo[4]));
    asm volatile("" : "+v"(woo[0]), "+v"(woo[1]), "+v"(woo[2]), "+v"(woo[3]), "+v"(woo[4]));
    auto dma_wq = [&](auto nc) {
        const unsigned m = 4 * nc + w, pr = m / 5, ph = m - 5 * pr, pair = pr > 9 ? 9 : pr;
        dma_piece<0>(lb + L_WQ + pair * 5120 + ph * 1024, sw, kstep(2 * pair) * 122880, wqo[nc % 5]);
    };
    auto dma_wo = [&](auto nc) { const unsigned s2 = 4 * (nc / 5) + w; dma_piece<nc % 5>(lb + L_WO + s2 * 5120, so, s2 * 40960, woo[nc % 5]); };

    b8 xf[20][2];
    f4 acc[2][5];
    LAS char* const xrd0 = lds + xr + i * 128 + ((g ^ (i >> 1)) << 4);
    LAS char* const xrd1 = lds + xr + i * 128 + (((4 + g) ^ (i >> 1)) << 4);
    LAS char* const wrd = lds + L_WKV + (h * 32 + (p & 1) * 8 + g1 * 4 + q) * 80 + (p >> 1) * 40;
    auto w_frag = [&](int it, int t) { return tr_frag(wrd + it * 5120 + 8 * t, wrd + it * 5120 + 1280 + 8 * t); };
    PhiW pwk, pwq;
    u64 epoch = 0;
    b8 wa[5], wb[5];
    static_for<10>([&](auto jc) {
        constexpr int j = jc;
        if (j == 0) WAIT_VM_BAR(9); else if (j == 8) WAIT_VM_BAR(8); else if (j == 9) WAIT_VM(8); else if (j == 1) { } else if (j & 1) WAIT_VM(13); else WAIT_VM_BAR(13);
        if (j == 0) STAMP(1);
#pragma unroll
        for (int t = 0; t < 2; ++t) xf[2 * j][t] = *(LAS b8*)(xrd0 + (j % 3) * 4096 + t * 2048);
        if ((j & 1) == 0) {
#pragma unroll
            for (int t = 0; t < 5; ++t) wa[t] = w_frag(2 * j, t);
        }
#pragma unroll
        for (int t = 0; t < 2; ++t) xf[2 * j + 1][t] = *(LAS b8*)(xrd1 + (j % 3) * 4096 + t * 2048);
        SB();
        static_for<5>([&](auto t) {
            wb[t] = w_frag(2 * j + 1, t);
            mfma16_va(j == 0, acc[0][t], wa[t], xf[2 * j][0]);
            mfma16_va(j == 0, acc[1][t], wa[t], xf[2 * j][1]);
            SB();
        });
        landed(xf[2 * j + 1][0], xf[2 * j + 1][1]);
        auto refill = [&](auto nc) {
            constexpr int n = nc;
            if constexpr (j < 7) {
                if constexpr (j & 1) { if constexpr (n < 5) dma_wkv((j + 3) / 2, nc); else if constexpr (n < 9) dma_x(j + 3, std::integral_constant<int, (n < 9 ? n - 5 : 0)>{}); }
                else if constexpr (n < 4) dma_x(j + 3, nc);
            } else if constexpr (j < 9 && n < 4) dma_wq(std::integral_constant<int, 4 * (j - 7) + n>{});
        };
        static_for<5>([&](auto t) {
            if ((j & 1) == 0) wa[t] = w_frag(2 * j + 2, t);
            mfma16_va(false, acc[0][t], wb[t], xf[2 * j + 1][0]);
            refill(std::integral_constant<int, 2 * t>{});
            mfma16_va(false, acc[1][t], wb[t], xf[2 * j + 1][1]);
            refill(std::integral_constant<int, 2 * t + 1>{});
            SB();
        });
        if (j == 1) {
            const u2 ew = *(LAS u2*)(lds + L_EPOCH);
            epoch = (((u64)(unsigned)__builtin_amdgcn_readfirstlane(ew[1]) << 32) | (unsigned)__builtin_amdgcn_readfirstlane(ew[0])) * 6364136223846793005ull + 1442695040888963407ull;
            const unsigned a_ = 2 * g1 + (p >> 1);
#pragma unroll
            for (int t = 0; t < 2; ++t) {
                const bool valid = (p & 1) == h && (t == 0 || a_ == 0);
                const unsigned base = L_WPHI + h * 800 + q * 40 + (16 * t + 4 * a_) * 2;
                s4 fk[5], fq[5];
#pragma unroll
                for (int m = 0; m < 5; ++m) fk[m] = tr_read(lds + (valid ? base + m * 160 : L_ZERO));
#pragma unroll
                for (int m = 0; m < 5; ++m) fq[m] = tr_read(lds + (valid ? base + (h ? (m + 1) % 5 : m) * 160 : L_ZERO));
                pwk.a0[t] = __builtin_bit_cast(b8, __builtin_shufflevector(fk[0], fk[1], 0, 1, 2, 3, 4, 5, 6, 7));
                pwk.a1[t] = __builtin_bit_cast(b8, __builtin_shufflevector(fk[2], fk[3], 0, 1, 2, 3, 4, 5, 6, 7));
                pwk.a2[t] = fk[4];
                pwq.a0[t] = __builtin_bit_cast(b8, __builtin_shufflevector(fq[0], fq[1], 0, 1, 2, 3, 4, 5, 6, 7));
                pwq.a1[t] = __builtin_bit_cast(b8, __builtin_shufflevector(fq[2], fq[3], 0, 1, 2, 3, 4, 5, 6, 7));
                pwq.a2[t] = fq[4];
            }
            SB();
        }
    });
    STAMP(2);

    {
        unsigned kk[10], vv[10], ph[20];
        asm volatile("" : "+v"(acc[0][0]), "+v"(acc[1][0]), "+v"(acc[0][1]), "+v"(acc[1][1]), "+v"(acc[0][2]), "+v"(acc[1][2]));
        static_for<10>([&](auto mc) {
            constexpr int m = mc;
            if (m == 6) { pin(kk[0], kk[1], kk[2], kk[3], kk[4], kk[5]); pin(acc[0][3], acc[1][3], acc[0][4], acc[1][4]); }
            const unsigned a0 = pack2(acc[0][m >> 1][2 * (m & 1)], acc[0][m >> 1][2 * (m & 1) + 1]);
            const unsigned a1 = pack2(acc[1][m >> 1][2 * (m & 1)], acc[1][m >> 1][2 * (m & 1) + 1]);
            const auto sw2 = __builtin_amdgcn_permlane16_swap(a0, a1, false, false);
            kk[m] = sw2[0]; vv[m] = sw2[1];
        });
        LAS u4* const vrow = (LAS u4*)(lds + xr + 5120 + l * 48);
        feature_map(pwk, kk, ph, [&](auto n) {
            if (n == 0) {
                vrow[0] = (u4){vv[0], vv[1], vv[2], vv[3]};
                vrow[1] = (u4){vv[4], vv[5], vv[6], vv[7]};
                vrow[2] = (u4){vv[8], vv[9], 0x00003f80u, 0u};
            }
            dma_wq(std::integral_constant<int, 8 + n>{});
        });
        LAS u4* const prow = (LAS u4*)(lds + xr + l * 80);
#pragma unroll
        for (int m = 0; m < 5; ++m) prow[m] = (u4){ph[4 * m], ph[4 * m + 1], ph[4 * m + 2], ph[4 * m + 3]};
    }
    LGKM_BAR();
    STAMP(3);
    const __amdgpu_buffer_rsrc_t rws = __builtin_amdgcn_make_buffer_rsrc(ws, 0, WS_END, 0x00020000);
    {
        const unsigned hh = w >> 1, tt = w & 1, rho = hh * 32 + 16 * h + 2 * (4 * g1 + q);
        LAS char* const pa = lds + L_XR + rho * 80 + 8 * p;
        LAS char* const pb = lds + L_XR + 5120 + rho * 48 + 32 * tt + 8 * p;
        b8 fa[4][3], fb[4];
#pragma unroll
        for (int st = 0; st < 4; ++st) {
            fb[st] = tr_frag(pb + st * 12288, pb + st * 12288 + 48);
#pragma unroll
            for (int f = 0; f < 3; ++f) fa[st][f] = tr_frag(pa + st * 12288 + 32 * f, pa + st * 12288 + 80 + 32 * f);
        }
        SB();
        f4 kv[3];
        static_for<4>([&](auto st) { static_for<3>([&](auto f) { mfma16_vv(st == 0, kv[f], fa[st][f], fb[st]); }); });
        const unsigned rec = WS_KVP + (hg * 16 + s) * 3840 + hh * 1920 + tt * 1280;
        const unsigned slot = tt ? 8 * g + i : l;
        const bool on = tt == 0 || i < 8;
        const unsigned big = 0x7ffffff0u;
        unsigned o0 = on ? slot * 16 : big, o1 = (on && g < 2) ? (tt ? 512 : 1024) + slot * 8 : big;
        asm volatile("s_nop 7" : "+v"(kv[0]), "+v"(kv[1]), "+v"(kv[2]), "+v"(o0), "+v"(o1));
        __builtin_amdgcn_raw_buffer_store_b128((u4){pack2(kv[0][0], kv[0][1]), pack2(kv[0][2], kv[0][3]), pack2(kv[1][0], kv[1][1]), pack2(kv[1][2], kv[1][3])}, rws, o0, rec, 16);
        __builtin_amdgcn_raw_buffer_store_b64((u2){pack2(kv[2][0], kv[2][1]), pack2(kv[2][2], kv[2][3])}, rws, o1, rec, 16);
    }
    WAIT_VM_BAR(2);
    STAMP(4);

    f4 aq[2][3];
    const GAS u64* const fl1 = (const GAS u64*)(ws + WS_FL1 + (hg * 16 + (l >> 2)) * 128 + (l & 3) * 8);
    {
        LAS char* const qb = lds + L_WQ + (16 * h + 2 * (4 * g1 + q)) * 80;
        LAS char* const q01 = qb + (p < 2 ? 8 * p : 32 + 8 * p);
        LAS char* const q2 = qb + ((p == 1 || p == 2) ? 40 : 32);
        auto q_frag = [&](int it, int t) { LAS char* const a = (t < 2 ? q01 + 16 * t : q2) + it * 2560; return tr_frag(a, a + 80); };
        b8 qf[3][3];
#pragma unroll
        for (int it = 0; it < 2; ++it)
#pragma unroll
            for (int t = 0; t < 3; ++t) qf[it][t] = q_frag(it, t);
        SB();
        static_for<20>([&](auto itc) {
            constexpr int it = itc;
            if (it == FLAG1_IT) {
                wait_vm<(FLAG1_IT < WO_IN_Q ? FLAG1_IT : WO_IN_Q)>();
                if (l == 0) set_flag((GAS u64*)(ws + WS_FL1 + (hg * 16 + s) * 128 + w * 8), epoch);
                SB();
            }
            static_for<3>([&](auto t) {
                if (it + 2 < 20) qf[(it + 2) % 3][t] = q_frag(it + 2, t);
                mfma16_va(it == 0, aq[0][t], qf[it % 3][t], xf[it][0]);
                if constexpr (t == 0 && it < WO_IN_Q) dma_wo(itc);
                mfma16_va(it == 0, aq[1][t], qf[it % 3][t], xf[it][1]);
                SB();
            });
        });
    }
    STAMP(5);
    u64 fv = load_flag(fl1);
    SB();
    u4 part[4][4];
    unsigned phq[20];
    {
        unsigned qq[10];
        asm volatile("s_nop 6" : "+v"(aq[0][0]), "+v"(aq[1][0]), "+v"(aq[0][1]), "+v"(aq[1][1]), "+v"(aq[0][2]), "+v"(aq[1][2]));
#pragma unroll
        for (int t = 0; t < 3; ++t)
#pragma unroll
            for (int e = 0; e < 2; ++e) {
                const auto sw2 = __builtin_amdgcn_permlane16_swap(pack2(aq[0][t][2 * e], aq[0][t][2 * e + 1]), pack2(aq[1][t][2 * e], aq[1][t][2 * e + 1]), false, false);
                qq[4 * t + e] = sw2[0];
                if (t < 2) qq[4 * t + 2 + e] = sw2[1];
            }
        feature_map(pwq, qq, phq, [&](auto n) {
            if constexpr (n == GATHER_HOOK) {
                wait_flags(fl1, epoch, fv);
                STAMP(6);
#pragma unroll
                for (int bi = 0; bi < 4; ++bi) {
                    const unsigned blk = (bi == 3 && w == 3) ? 14 : w + 4 * bi;
#pragma unroll
                    for (int st = 0; st < 4; ++st)
                        part[bi][st] = __builtin_amdgcn_raw_buffer_load_b128(rws, g * 3840 + i * 16, WS_KVP + (hg * 16 + 4 * st) * 3840 + blk * 256, 16);
                }
            }
            if constexpr (n == 4) static_for<25 - WO_IN_Q>([&](auto m) { dma_wo(std::integral_constant<int, WO_IN_Q + m>{}); });
        });
#pragma unroll
        for (int m = 0; m < 20; m += 4) asm volatile("" : "+v"(phq[m]), "+v"(phq[m + 1]), "+v"(phq[m + 2]), "+v"(phq[m + 3]));
    }
    SB();
    STAMP(7);
    {
        b8 sel[2];
#pragma unroll
        for (int e = 0; e < 2; ++e) {
            u4 v;
#pragma unroll
            for (int m = 0; m < 4; ++m) v[m] = i == 8 * e + 2 * m ? 0x00003f80u : i == 8 * e + 2 * m + 1 ? 0x3f800000u : 0u;
            sel[e] = __builtin_bit_cast(b8, v);
        }
        asm volatile("s_nop 1" : "+v"(sel[0]), "+v"(sel[1]));
        f4 sum[2];
        static_for<2>([&](auto pr) { static_for<8>([&](auto n) { mfma16_vv(n == 0, sum[pr], sel[n / 4], part[2 * pr + n / 4][n % 4]); }); });
        asm volatile("" : "+v"(sum[0]));
        *(LAS u2*)(lds + kvt_dst[0]) = (u2){pack2(sum[0][0], sum[0][1]), pack2(sum[0][2], sum[0][3])};
        asm volatile("s_nop 4" : "+v"(sum[1]) :: "memory");
        *(LAS u2*)(lds + kvt_dst[1]) = (u2){pack2(sum[1][0], sum[1][1]), pack2(sum[1][2], sum[1][3])};
    }
    WAIT_VM_BAR(0);
    STAMP(8);
    {
        b8 ka[5], kb[5];
#pragma unroll
        for (int st = 0; st < 5; ++st) { kb[st] = *(LAS b8*)(lds + k1a + st * k1s); ka[st] = *(LAS b8*)(lds + k0a + st * k0s); }
        SB();
        f16v n0, n1;
        static_for<5>([&](auto st) {
            const b8 bq = as_frag(phq[4 * st], phq[4 * st + 1], phq[4 * st + 2], phq[4 * st + 3]);
            mfma32_pad(st == 0, n1, kb[st], bq);
        });
        static_for<5>([&](auto st) {
            const b8 bq = as_frag(phq[4 * st], phq[4 * st + 1], phq[4 * st + 2], phq[4 * st + 3]);
            mfma32_pad(st == 0, n0, ka[st], bq);
        });
        asm volatile("" : "+v"(n1));
        float inv = __builtin_amdgcn_rcpf(n1[4] + 1e-6f);
        unsigned o[10];
        o[8] = pack2(n1[0] * inv, n1[1] * inv);
        o[9] = pack2(n1[2] * inv, n1[3] * inv);
        const unsigned ob = WS_O + (s * 8 + 2 * w) * 20480 + hg * 1280;
        const unsigned vo = (c >> 4) * 20480 + i * 16;
        __builtin_amdgcn_raw_buffer_store_b64((u2){o[8], o[9]}, rws, vo + 512 + h * 8, ob, 16);
        asm volatile("s_nop 3" : "+v"(n0), "+v"(inv) :: "memory");
#pragma unroll
        for (int m = 0; m < 8; ++m) o[m] = pack2(n0[2 * m] * inv, n0[2 * m + 1] * inv);
        __builtin_amdgcn_raw_buffer_store_b128((u4){o[0], o[1], o[2], o[3]}, rws, vo + h * 768, ob, 16);
        __builtin_amdgcn_raw_buffer_store_b128((u4){o[4], o[5], o[6], o[7]}, rws, vo + h * 768 + 256, ob, 16);
        SB();
        asm volatile("s_nop 1" ::: "memory");
        SB();
    }
    STAMP(9);

    LAS char* const word = lds + L_WO + (16 * h + 4 * g1 + q) * 160 + 8 * p;
    auto o_frag = [&](int s2, int t) { return tr_frag(word + s2 * 5120 + 32 * t, word + s2 * 5120 + 1280 + 32 * t); };
    b8 wf[20][5];
    static_for<16>([&](auto s2) {
        if (s2 == PRE_BEFORE_FLAG) {
            WAIT_VM(0);
            if (l == 0) set_flag((GAS u64*)(ws + WS_FL2 + ((s * 4 + w) * 16 + hg) * 128), epoch);
            STAMP(10);
        }
#pragma unroll
        for (int t = 0; t < 5; ++t) wf[s2][t] = o_frag(s2, t);
        SB();
    });
    const GAS u64* const fl2 = (const GAS u64*)(ws + WS_FL2 + ((rt * 2 + (w >> 1)) * 16 + i) * 128);
    wait_flags(fl2, epoch, load_flag(fl2));
    STAMP(11);
    u4 of[20];
#pragma unroll
    for (int s2 = 0; s2 < 20; ++s2) of[s2] = __builtin_amdgcn_raw_buffer_load_b128(rws, l * 16, WS_O + (rt * 4 + w) * 20480 + s2 * 1024, 16);
    SB();
    f4 ao[5];
    static_for<20>([&](auto s2c) {
        constexpr int s2 = s2c;
        static_for<5>([&](auto t) {
            if constexpr (s2 >= 13 && s2 < 17) wf[s2 + 3][t] = o_frag(s2 + 3, t);
            if constexpr (s2 < 12) mfma16_av(s2 == 0, ao[t], wf[s2][t], of[s2]);
            else mfma16_vv(false, ao[t], wf[s2][t], of[s2]);
            SB();
        });
    });
    STAMP(12);
    {
        GAS char* const orow = (GAS char*)out + (rt * 64 + 16 * w + i) * 1280 + ct * 160;
        asm volatile("s_nop 3" : "+v"(ao[0]), "+v"(ao[1]));
        static_for<2>([&](auto tc) {
            constexpr int t = 2 * tc;
            if (t == 2) { SB(); pin(ao[2], ao[3], ao[4]); }
            const auto lo = __builtin_amdgcn_permlane16_swap(pack2(ao[t][0], ao[t][1]), pack2(ao[t + 1][0], ao[t + 1][1]), false, false);
            const auto hi = __builtin_amdgcn_permlane16_swap(pack2(ao[t][2], ao[t][3]), pack2(ao[t + 1][2], ao[t + 1][3]), false, false);
            *(GAS u4*)(orow + 32 * t + 32 * g1 + 16 * h) = (u4){lo[0], hi[0], lo[1], hi[1]};
        });
        *(GAS u2*)(orow + 128 + 8 * g) = (u2){pack2(ao[4][0], ao[4][1]), pack2(ao[4][2], ao[4][3])};
    }
    if (b == 0 && threadIdx.x == 0) set_flag((GAS u64*)(ws + WS_EPOCH), epoch);
    STAMP(13);
    WAIT_VM(0);
    STAMP(14);
    const u64 c1 = __builtin_amdgcn_s_memtime();
    ts[13] = ts[0] + (c1 - c0) / 24;
    if (l == 0) {
        GAS u64* dg = (GAS u64*)(ws + WS_DIAG) + (b * 4 + w) * 16;
#pragma unroll
        for (int k = 0; k < 16; ++k) dg[k] = ts[k];
    }
}

extern "C" void kernel_launch(void* const* d_in, const int* in_sizes, int n_in, void* d_out, int out_size, void* d_ws,
                              size_t ws_size, hipStream_t stream) {
    hedgehog_fused<<<256, 256, 0, stream>>>((const char*)d_in[0], (const char*)d_in[1], (const char*)d_in[2],
                                            (const char*)d_in[3], (char*)d_out, (char*)d_ws);
}
```

```cpp
#define MID_LDS 0
#define MID_VGPR 0
#include <hip/hip_runtime.h>
#include <hip/hip_bf16.h>
#include <utility>

#define DI __device__ __forceinline__
#define GAS __attribute__((address_space(1)))
#define LAS __attribute__((address_space(3)))
#define CAS __attribute__((address_space(4)))
typedef unsigned long long u64;
typedef unsigned u2 __attribute__((ext_vector_type(2)));
typedef unsigned u4 __attribute__((ext_vector_type(4)));
typedef short s4 __attribute__((ext_vector_type(4)));
typedef short s8 __attribute__((ext_vector_type(8)));
typedef __bf16 b2 __attribute__((ext_vector_type(2)));
typedef __bf16 b8 __attribute__((ext_vector_type(8)));
typedef float f2 __attribute__((ext_vector_type(2)));
typedef float f4 __attribute__((ext_vector_type(4)));
typedef float f16v __attribute__((ext_vector_type(16)));

#define DIAG 0

constexpr unsigned WS_EPOCH = 0;
constexpr unsigned WS_FL1 = 4096;
constexpr unsigned WS_FL2 = 65536;
constexpr unsigned WS_KVP = 262144;
constexpr unsigned WS_O = 1310720;
constexpr unsigned WS_END = WS_O + 2048 * 640 * 2;
constexpr unsigned WS_DIAG = 8u << 20;

constexpr unsigned L_WKV = 0;
constexpr unsigned L_WQ = 0;
constexpr unsigned L_WO = 51200;
constexpr unsigned L_XR = 102400;
constexpr unsigned L_WPHI = 153600;
constexpr unsigned L_ZERO = 155648;
constexpr unsigned L_JUNK = 155904;
constexpr unsigned L_EPOCH = 156160;
constexpr unsigned L_KVT = 156416;
constexpr unsigned L_SIZE = 163840;

constexpr unsigned SPIN_CAP = 1u << 21;

DI u4 make_srd(const void* p, unsigned bytes) {
    const u64 a = (u64)p;
    return (u4){(unsigned)a, (unsigned)(a >> 32) & 0xffffu, bytes, 0x00020000u};
}
DI void set_m0(unsigned lds) { asm volatile("s_mov_b32 m0, %0\n\ts_nop 0" :: "s"(lds) : "memory"); }
template <int OFF> DI void dma(u4 srd, unsigned soff, unsigned voff) {
    asm volatile("buffer_load_dwordx4 %2, %0, %1 offen offset:%3 lds" :: "s"(srd), "s"(soff), "v"(voff), "i"(OFF) : "memory");
}
template <int PH> DI void dma_piece(unsigned lds, u4 srd, unsigned soff, unsigned voff) {
    if (PH == 0) set_m0(lds);
    if (PH == 4) set_m0(lds + 4096);
    dma<(PH & 3) * 1024>(srd, soff, voff);
}
DI void touch(unsigned lds, u4 srd, unsigned voff) {
    asm volatile("s_mov_b32 m0, %0\n\ts_nop 0\n\tbuffer_load_dword %2, %1, 0 offen lds" :: "s"(lds), "s"(srd), "v"(voff) : "memory");
}
template <class F, int... I> DI void static_for_impl(F&& f, std::integer_sequence<int, I...>) { (f(std::integral_constant<int, I>{}), ...); }
template <int N, class F> DI void static_for(F&& f) { static_for_impl(f, std::make_integer_sequence<int, N>{}); }
#define SB() __builtin_amdgcn_sched_barrier(0)
#define WAIT_VM(n) asm volatile("s_waitcnt vmcnt(" #n ")" ::: "memory")
#define WAIT_VM_BAR(n) asm volatile("s_waitcnt vmcnt(" #n ") lgkmcnt(0)\n\ts_barrier" ::: "memory")
#define WAIT_LGKM() asm volatile("s_waitcnt lgkmcnt(0)" ::: "memory")
#define LGKM_BAR() asm volatile("s_waitcnt lgkmcnt(0)\n\ts_barrier" ::: "memory")

DI s4 tr_read(LAS char* p) { return __builtin_amdgcn_ds_read_tr16_b64_v4i16((LAS s4*)p); }
DI b8 tr_frag(LAS char* p0, LAS char* p1) {
    const s4 lo = tr_read(p0), hi = tr_read(p1);
    return __builtin_bit_cast(b8, __builtin_shufflevector(lo, hi, 0, 1, 2, 3, 4, 5, 6, 7));
}
DI unsigned pack2(float a, float b) { return __builtin_bit_cast(unsigned, __builtin_convertvector((f2){a, b}, b2)); }
DI b8 as_frag(unsigned a, unsigned b, unsigned c, unsigned d) { return __builtin_bit_cast(b8, (u4){a, b, c, d}); }
#define DEF_MFMA16(name, ca, cb)                                                                                          \
    template <class A, class B> DI void name(bool first, f4& acc, const A& a, const B& b) {                              \
        if (first) asm volatile("v_mfma_f32_16x16x32_bf16 %0, %1, %2, 0" : "=&v"(acc) : ca(a), cb(b));                    \
        else asm volatile("v_mfma_f32_16x16x32_bf16 %0, %1, %2, %0" : "+v"(acc) : ca(a), cb(b));                          \
    }
DEF_MFMA16(mfma16_vv, "v", "v")
DEF_MFMA16(mfma16_va, "v", "a")
DEF_MFMA16(mfma16_av, "a", "v")
DI void mfma32_pad(bool first, f16v& acc, const b8& a, const b8& b) {
    if (first) asm volatile("s_nop 1\n\tv_mfma_f32_32x32x16_bf16 %0, %1, %2, 0" : "=&v"(acc) : "v"(a), "v"(b));
    else asm volatile("s_nop 1\n\tv_mfma_f32_32x32x16_bf16 %0, %1, %2, %0" : "+v"(acc) : "v"(a), "v"(b));
}
DI void mfma32k8_pad(f16v& acc, const s4& a, const s4& b) {
    asm volatile("s_nop 1\n\tv_mfma_f32_32x32x8_bf16 %0, %1, %2, %0" : "+v"(acc) : "v"(a), "v"(b));
}
template <class T> DI void pin(T& a) { asm volatile("" : "+v"(a)); }
template <class T, class... R> DI void pin(T& a, R&... r) { pin(a); pin(r...); }
DI void landed(const b8& a, const b8& b) { asm volatile("" :: "a"(a), "a"(b) : "memory"); }
template <int N> DI void wait_vm() { asm volatile("s_waitcnt vmcnt(%0)" :: "i"(N) : "memory"); }

struct PhiW { b8 a0[2], a1[2]; s4 a2[2]; };

template <class F> DI void feature_map(const PhiW& w, const unsigned (&in)[10], unsigned (&out)[20], F&& hook) {
    const b8 b0 = as_frag(in[0], in[1], in[2], in[3]), b1 = as_frag(in[4], in[5], in[6], in[7]);
    const s4 b2_ = __builtin_bit_cast(s4, (u2){in[8], in[9]});
    f16v d0, d1;
    mfma32_pad(true, d0, w.a0[0], b0);
    mfma32_pad(false, d0, w.a1[0], b1);
    mfma32k8_pad(d0, w.a2[0], b2_);
    mfma32_pad(true, d1, w.a0[1], b0);
    mfma32_pad(false, d1, w.a1[1], b1);
    mfma32k8_pad(d1, w.a2[1], b2_);
    hook(std::integral_constant<int, 0>{});
    asm volatile("s_nop 7" : "+v"(d0));
    float p[20];
#pragma unroll
    for (int r = 0; r < 16; ++r) p[r] = d0[r];
    float mx = p[0], mn = p[0];
#pragma unroll
    for (int r = 1; r < 15; ++r) { mx = __builtin_elementwise_maximum(mx, p[r]); mn = __builtin_elementwise_minimum(mn, p[r]); }
    asm volatile("" : "+v"(mx), "+v"(mn), "+v"(d1));
#pragma unroll
    for (int r = 16; r < 20; ++r) p[r] = d1[r - 16];
#pragma unroll
    for (int r = 15; r < 20; ++r) { mx = __builtin_elementwise_maximum(mx, p[r]); mn = __builtin_elementwise_minimum(mn, p[r]); }
    constexpr float L = 1.4426950408889634f;
    const f2 cp = {-mx * L, -mx * L}, cn = {mn * L, mn * L};
    f2 e[10], f[10];
    static_for<10>([&](auto mc) {
        constexpr int m = mc;
        if (m == 0) { SB(); hook(std::integral_constant<int, 1>{}); SB(); }
        if (m == 4) { SB(); hook(std::integral_constant<int, 2>{}); SB(); }
        if (m == 8) { SB(); hook(std::integral_constant<int, 3>{}); SB(); }
        const f2 x = {p[2 * m], p[2 * m + 1]};
        const f2 a = __builtin_elementwise_fma(x, (f2){L, L}, cp), b = __builtin_elementwise_fma(x, (f2){-L, -L}, cn);
        e[m] = (f2){__builtin_amdgcn_exp2f(a[0]), __builtin_amdgcn_exp2f(a[1])};
        f[m] = (f2){__builtin_amdgcn_exp2f(b[0]), __builtin_amdgcn_exp2f(b[1])};
    });
    f2 se = (e[0] + e[1]) + (e[2] + e[3]) + ((e[4] + e[5]) + (e[6] + e[7])) + (e[8] + e[9]);
    f2 sf = (f[0] + f[1]) + (f[2] + f[3]) + ((f[4] + f[5]) + (f[6] + f[7])) + (f[8] + f[9]);
    const float re = __builtin_amdgcn_rcpf(se[0] + se[1]), rf = __builtin_amdgcn_rcpf(sf[0] + sf[1]);
    SB(); hook(std::integral_constant<int, 4>{}); SB();
#pragma unroll
    for (int m = 0; m < 10; ++m) {
        const f2 a = e[m] * (f2){re, re}, b = f[m] * (f2){rf, rf};
        out[m] = pack2(a[0], a[1]);
        out[10 + m] = pack2(b[0], b[1]);
    }
}

DI u64 load_flag(const GAS u64* p) { return __hip_atomic_load(p, __ATOMIC_RELAXED, __HIP_MEMORY_SCOPE_AGENT); }
DI void wait_flags(const GAS u64* p, u64 epoch, u64 v) {
#pragma unroll 1
    for (unsigned spins = 0; spins < SPIN_CAP && !__all(v == epoch); ++spins) v = load_flag(p);
    asm volatile("" ::: "memory");
}
DI void set_flag(GAS u64* p, u64 epoch) { __hip_atomic_store(p, epoch, __ATOMIC_RELAXED, __HIP_MEMORY_SCOPE_AGENT); }

#define STAMP(i) do { } while (0)

__global__ __launch_bounds__(256, 1) void hedgehog_fused(const char* __restrict__ x, const char* __restrict__ wqkv,
                                                         const char* __restrict__ wphi, const char* __restrict__ wo,
                                                         char* __restrict__ out, char* __restrict__ ws) {
    __shared__ __attribute__((aligned(16))) char lds_raw[L_SIZE];
    LAS char* const lds = (LAS char*)lds_raw;
    const unsigned lb = (unsigned)(size_t)lds;
    STAMP(0);
    const unsigned b = blockIdx.x, xcd = b & 7, r = b >> 3;
    {
        u64 pc;
        asm volatile("s_getpc_b64 %0" : "=s"(pc));
        if (threadIdx.x < 8) touch(lb + L_JUNK, make_srd((const void*)(pc & ~127ull), KERNEL_BYTES), (r * 8 + threadIdx.x) * 128);
    }
    SB();
    const unsigned l = threadIdx.x & 63, w = __builtin_amdgcn_readfirstlane(threadIdx.x >> 6);
    const unsigned g = l >> 4, i = l & 15, h = l >> 5, c = l & 31, q = (l >> 2) & 3, p = l & 3, g1 = g & 1;
    const unsigned s = (xcd & 1) * 8 + (r & 7), hg = (xcd >> 1) * 4 + (r >> 3);
    const unsigned rt = xcd * 4 + (r & 3), ct = r >> 2;
    const unsigned rot = ((3 * s + hg) % 10) * 2;
    auto kstep = [&](unsigned it) { const unsigned k = rot + it; return k >= 20 ? k - 20 : k; };

    unsigned xo[4], wko[5];
#pragma unroll
    for (int m = 0; m < 4; ++m) {
        const unsigned row = 8 * m + (l >> 3);
        xo[m] = (s * 128 + 32 * w + row) * 1280 + (((l & 7) ^ ((row >> 1) & 7)) << 4) - 1024 * m;
    }
    asm volatile("" :: "s"(x), "s"(wqkv), "s"(wphi), "s"(wo), "s"(out), "s"(ws));
    STAMP(15);
    const u4 sx = make_srd(x, 2048 * 1280), sw = make_srd(wqkv, 640 * 3840), sp = make_srd(wphi, 32 * 800), so = make_srd(wo, 640 * 1280);
    const unsigned xr = L_XR + w * 12288;
    auto dma_x = [&](unsigned j, auto mc) { dma_piece<mc>(lb + xr + (j % 3) * 4096, sx, kstep(2 * j) * 64, xo[mc]); };
    static_for<4>([&](auto m) { dma_x(0, m); });
    static_for<4>([&](auto m) { dma_x(1, m); });
    SB();
#pragma unroll
    for (int ph = 0; ph < 5; ++ph) {
        const unsigned n = 64 * ph + l, u = n / 5, ch = n - 5 * u;
        const unsigned row = 16 * (u >> 5) + 8 * ((u >> 2) & 1) + 4 * ((u >> 4) & 1) + (u & 3);
        wko[ph] = row * 3840 + (1 + ((u >> 3) & 1)) * 1280 + hg * 80 + ch * 16 - (ph < 4 ? 1024 * ph : 0);
    }
    auto dma_wkv = [&](unsigned cc, auto ph) { dma_piece<ph>(lb + L_WKV + (4 * cc + w) * 5120, sw, kstep(4 * cc + w) * 122880, wko[ph]); };
    static_for<5>([&](auto ph) { dma_wkv(0, ph); });
    if (w < 2) dma_piece<0>(lb + L_WPHI + w * 1024, sp, hg * 1600 + w * 1024, l * 16);
    else if (w == 2) touch(lb + L_EPOCH, make_srd(ws, WS_END), l < 2 ? WS_EPOCH + 4 * l : (l & 3) << 20);
    else touch(lb + L_JUNK, make_srd(out, 2048 * 1280), rt * 81920 + (l & 1) * 81916);
    static_for<5>([&](auto ph) { dma_wkv(1, ph); });
    static_for<4>([&](auto m) { dma_x(2, m); });
    SB();
    if (l < 16) *(LAS unsigned*)(lds + L_ZERO + l * 4) = 0;

    unsigned kvt_dst[2];
#pragma unroll
    for (int pr = 0; pr < 2; ++pr) {
        const unsigned bi = 2 * pr + h, blk = (bi == 3 && w == 3) ? 14 : w + 4 * bi;
        const unsigned ci = 16 * blk + i, hh = ci >= 120, cc = ci - 120 * hh;
        const bool t1 = cc >= 80;
        const unsigned m = t1 ? cc - 80 : cc, sh = t1 ? 3 : 4, nfull = t1 ? 32 : 64;
        const bool f2_ = m >= nfull;
        const unsigned idx = f2_ ? 2 * (m - nfull) + g1 : m;
        const unsigned d = (t1 ? 16 : 0) + (idx & ((1u << sh) - 1)), f0 = (f2_ ? 32 : 16 * g1) + 4 * (idx >> sh);
        kvt_dst[pr] = L_KVT + hh * 1920 + d * 80 + f0 * 2;
    }
    unsigned wqo[5], woo[5];
#pragma unroll
    for (int m = 0; m < 5; ++m) {
        const unsigned ph = (w + 4 * m) % 5;
        const unsigned n = 64 * ph + l, u = n / 5, ch = n - 5 * u;
        const unsigned row = 32 * (u >> 5) + 16 * ((u >> 4) & 1) + 8 * ((u >> 3) & 1) + 4 * (u & 1) + ((u >> 1) & 3);
        wqo[m] = row * 3840 + hg * 80 + ch * 16;
    }
#pragma unroll
    for (int ph = 0; ph < 5; ++ph) {
        const unsigned n = 64 * ph + l, u = n / 10, ch = n - 10 * u;
        const unsigned row = 16 * (u >> 4) + 8 * ((u >> 2) & 1) + 4 * ((u >> 3) & 1) + (u & 3);
        woo[ph] = row * 1280 + ct * 160 + ch * 16 - (ph < 4 ? 1024 * ph : 0);
    }
    unsigned k0a, k1a, k0s, k1s;
    {
        const unsigned a_ = c >> 3, hv = (c >> 2) & 1, bb = c & 3;
        const unsigned d0 = 4 * h + 4 * a_ + bb, d1 = a_ ? 20 + bb : (h ? bb : 16 + bb);
        const bool v0 = hv == h, v1 = hv == h && a_ < 2;
        k0a = v0 ? L_KVT + h * 1920 + d0 * 80 : L_ZERO; k0s = v0 ? 16 : 0;
        k1a = v1 ? L_KVT + h * 1920 + d1 * 80 : L_ZERO; k1s = v1 ? 16 : 0;
    }
    asm volatile("" : "+v"(kvt_dst[0]), "+v"(kvt_dst[1]), "+v"(k0a), "+v"(k1a), "+v"(k0s), "+v"(k1s));
    asm volatile("" : "+v"(wqo[0]), "+v"(wqo[1]), "+v"(wqo[2]), "+v"(wqo[3]), "+v"(wqo[4]));
    asm volatile("" : "+v"(woo[0]), "+v"(woo[1]), "+v"(woo[2]), "+v"(woo[3]), "+v"(woo[4]));
    auto dma_wq = [&](auto nc) {
        const unsigned m = 4 * nc + w, pr = m / 5, ph = m - 5 * pr, pair = pr > 9 ? 9 : pr;
        dma_piece<0>(lb + L_WQ + pair * 5120 + ph * 1024, sw, kstep(2 * pair) * 122880, wqo[nc % 5]);
    };
    auto dma_wo = [&](auto nc) { const unsigned s2 = 4 * (nc / 5) + w; dma_piece<nc % 5>(lb + L_WO + s2 * 5120, so, s2 * 40960, woo[nc % 5]); };

    b8 xf[20][2];
    f4 acc[2][5];
    LAS char* const xrd0 = lds + xr + i * 128 + ((g ^ (i >> 1)) << 4);
    LAS char* const xrd1 = lds + xr + i * 128 + (((4 + g) ^ (i >> 1)) << 4);
    LAS char* const wrd = lds + L_WKV + (h * 32 + (p & 1) * 8 + g1 * 4 + q) * 80 + (p >> 1) * 40;
    auto w_frag = [&](int it, int t) { return tr_frag(wrd + it * 5120 + 8 * t, wrd + it * 5120 + 1280 + 8 * t); };
    PhiW pwk, pwq;
    u64 epoch = 0;
    b8 wa[5], wb[5];
    static_for<10>([&](auto jc) {
        constexpr int j = jc;
        if (j == 0) WAIT_VM_BAR(9); else if (j == 8) WAIT_VM_BAR(8); else if (j == 9) WAIT_VM(8); else if (j == 1) { } else if (j & 1) WAIT_VM(13); else WAIT_VM_BAR(13);
        if (j == 0) STAMP(1);
#pragma unroll
        for (int t = 0; t < 2; ++t) xf[2 * j][t] = *(LAS b8*)(xrd0 + (j % 3) * 4096 + t * 2048);
        if ((j & 1) == 0) {
#pragma unroll
            for (int t = 0; t < 5; ++t) wa[t] = w_frag(2 * j, t);
        }
#pragma unroll
        for (int t = 0; t < 2; ++t) xf[2 * j + 1][t] = *(LAS b8*)(xrd1 + (j % 3) * 4096 + t * 2048);
        SB();
        static_for<5>([&](auto t) {
            wb[t] = w_frag(2 * j + 1, t);
            mfma16_va(j == 0, acc[0][t], wa[t], xf[2 * j][0]);
            mfma16_va(j == 0, acc[1][t], wa[t], xf[2 * j][1]);
            SB();
        });
        landed(xf[2 * j + 1][0], xf[2 * j + 1][1]);
        auto refill = [&](auto nc) {
            constexpr int n = nc;
            if constexpr (j < 7) {
                if constexpr (j & 1) { if constexpr (n < 5) dma_wkv((j + 3) / 2, nc); else if constexpr (n < 9) dma_x(j + 3, std::integral_constant<int, (n < 9 ? n - 5 : 0)>{}); }
                else if constexpr (n < 4) dma_x(j + 3, nc);
            } else if constexpr (j < 9 && n < 4) dma_wq(std::integral_constant<int, 4 * (j - 7) + n>{});
        };
        static_for<5>([&](auto t) {
            if ((j & 1) == 0) wa[t] = w_frag(2 * j + 2, t);
            mfma16_va(false, acc[0][t], wb[t], xf[2 * j + 1][0]);
            refill(std::integral_constant<int, 2 * t>{});
            mfma16_va(false, acc[1][t], wb[t], xf[2 * j + 1][1]);
            refill(std::integral_constant<int, 2 * t + 1>{});
            SB();
        });
        if (j == 1) {
            const u2 ew = *(LAS u2*)(lds + L_EPOCH);
            epoch = (((u64)(unsigned)__builtin_amdgcn_readfirstlane(ew[1]) << 32) | (unsigned)__builtin_amdgcn_readfirstlane(ew[0])) * 6364136223846793005ull + 1442695040888963407ull;
            const unsigned a_ = 2 * g1 + (p >> 1);
#pragma unroll
            for (int t = 0; t < 2; ++t) {
                const bool valid = (p & 1) == h && (t == 0 || a_ == 0);
                const unsigned base = L_WPHI + h * 800 + q * 40 + (16 * t + 4 * a_) * 2;
                s4 fk[5], fq[5];
#pragma unroll
                for (int m = 0; m < 5; ++m) fk[m] = tr_read(lds + (valid ? base + m * 160 : L_ZERO));
#pragma unroll
                for (int m = 0; m < 5; ++m) fq[m] = tr_read(lds + (valid ? base + (h ? (m + 1) % 5 : m) * 160 : L_ZERO));
                pwk.a0[t] = __builtin_bit_cast(b8, __builtin_shufflevector(fk[0], fk[1], 0, 1, 2, 3, 4, 5, 6, 7));
                pwk.a1[t] = __builtin_bit_cast(b8, __builtin_shufflevector(fk[2], fk[3], 0, 1, 2, 3, 4, 5, 6, 7));
                pwk.a2[t] = fk[4];
                pwq.a0[t] = __builtin_bit_cast(b8, __builtin_shufflevector(fq[0], fq[1], 0, 1, 2, 3, 4, 5, 6, 7));
                pwq.a1[t] = __builtin_bit_cast(b8, __builtin_shufflevector(fq[2], fq[3], 0, 1, 2, 3, 4, 5, 6, 7));
                pwq.a2[t] = fq[4];
            }
            SB();
        }
    });
    STAMP(2);

    {
        unsigned kk[10], vv[10], ph[20];
        asm volatile("" : "+v"(acc[0][0]), "+v"(acc[1][0]), "+v"(acc[0][1]), "+v"(acc[1][1]), "+v"(acc[0][2]), "+v"(acc[1][2]));
        static_for<10>([&](auto mc) {
            constexpr int m = mc;
            if (m == 6) { pin(kk[0], kk[1], kk[2], kk[3], kk[4], kk[5]); pin(acc[0][3], acc[1][3], acc[0][4], acc[1][4]); }
            const unsigned a0 = pack2(acc[0][m >> 1][2 * (m & 1)], acc[0][m >> 1][2 * (m & 1) + 1]);
            const unsigned a1 = pack2(acc[1][m >> 1][2 * (m & 1)], acc[1][m >> 1][2 * (m & 1) + 1]);
            const auto sw2 = __builtin_amdgcn_permlane16_swap(a0, a1, false, false);
            kk[m] = sw2[0]; vv[m] = sw2[1];
        });
        LAS u4* const vrow = (LAS u4*)(lds + xr + 5120 + l * 48);
        feature_map(pwk, kk, ph, [&](auto n) {
            if (n == 0) {
                vrow[0] = (u4){vv[0], vv[1], vv[2], vv[3]};
                vrow[1] = (u4){vv[4], vv[5], vv[6], vv[7]};
                vrow[2] = (u4){vv[8], vv[9], 0x00003f80u, 0u};
            }
            dma_wq(std::integral_constant<int, 8 + n>{});
        });
        LAS u4* const prow = (LAS u4*)(lds + xr + l * 80);
#pragma unroll
        for (int m = 0; m < 5; ++m) prow[m] = (u4){ph[4 * m], ph[4 * m + 1], ph[4 * m + 2], ph[4 * m + 3]};
    }
    LGKM_BAR();
    STAMP(3);
    const __amdgpu_buffer_rsrc_t rws = __builtin_amdgcn_make_buffer_rsrc(ws, 0, WS_END, 0x00020000);
    {
        const unsigned hh = w >> 1, tt = w & 1, rho = hh * 32 + 16 * h + 2 * (4 * g1 + q);
        LAS char* const pa = lds + L_XR + rho * 80 + 8 * p;
        LAS char* const pb = lds + L_XR + 5120 + rho * 48 + 32 * tt + 8 * p;
        b8 fa[4][3], fb[4];
#pragma unroll
        for (int st = 0; st < 4; ++st) {
            fb[st] = tr_frag(pb + st * 12288, pb + st * 12288 + 48);
#pragma unroll
            for (int f = 0; f < 3; ++f) fa[st][f] = tr_frag(pa + st * 12288 + 32 * f, pa + st * 12288 + 80 + 32 * f);
        }
        SB();
        f4 kv[3];
        static_for<4>([&](auto st) { static_for<3>([&](auto f) { mfma16_vv(st == 0, kv[f], fa[st][f], fb[st]); }); });
        const unsigned rec = WS_KVP + (hg * 16 + s) * 3840 + hh * 1920 + tt * 1280;
        const unsigned slot = tt ? 8 * g + i : l;
        const bool on = tt == 0 || i < 8;
        const unsigned big = 0x7ffffff0u;
        unsigned o0 = on ? slot * 16 : big, o1 = (on && g < 2) ? (tt ? 512 : 1024) + slot * 8 : big;
        asm volatile("s_nop 7" : "+v"(kv[0]), "+v"(kv[1]), "+v"(kv[2]), "+v"(o0), "+v"(o1));
        __builtin_amdgcn_raw_buffer_store_b128((u4){pack2(kv[0][0], kv[0][1]), pack2(kv[0][2], kv[0][3]), pack2(kv[1][0], kv[1][1]), pack2(kv[1][2], kv[1][3])}, rws, o0, rec, 16);
        __builtin_amdgcn_raw_buffer_store_b64((u2){pack2(kv[2][0], kv[2][1]), pack2(kv[2][2], kv[2][3])}, rws, o1, rec, 16);
    }
    WAIT_VM_BAR(2);
    STAMP(4);

    f4 aq[2][3];
    const GAS u64* const fl1 = (const GAS u64*)(ws + WS_FL1 + (hg * 16 + (l >> 2)) * 128 + (l & 3) * 8);
    {
        LAS char* const qb = lds + L_WQ + (16 * h + 2 * (4 * g1 + q)) * 80;
        LAS char* const q01 = qb + (p < 2 ? 8 * p : 32 + 8 * p);
        LAS char* const q2 = qb + ((p == 1 || p == 2) ? 40 : 32);
        auto q_frag = [&](int it, int t) { LAS char* const a = (t < 2 ? q01 + 16 * t : q2) + it * 2560; return tr_frag(a, a + 80); };
        b8 qf[3][3];
#pragma unroll
        for (int it = 0; it < 2; ++it)
#pragma unroll
            for (int t = 0; t < 3; ++t) qf[it][t] = q_frag(it, t);
        SB();
        static_for<20>([&](auto itc) {
            constexpr int it = itc;
            if (it == FLAG1_IT) {
                wait_vm<(FLAG1_IT < WO_IN_Q ? FLAG1_IT : WO_IN_Q)>();
                if (l == 0) set_flag((GAS u64*)(ws + WS_FL1 + (hg * 16 + s) * 128 + w * 8), epoch);
                SB();
            }
            static_for<3>([&](auto t) {
                if (it + 2 < 20) qf[(it + 2) % 3][t] = q_frag(it + 2, t);
                mfma16_va(it == 0, aq[0][t], qf[it % 3][t], xf[it][0]);
                if constexpr (t == 0 && it < WO_IN_Q) dma_wo(itc);
                mfma16_va(it == 0, aq[1][t], qf[it % 3][t], xf[it][1]);
                SB();
            });
        });
    }
    STAMP(5);
    u64 fv = load_flag(fl1);
    SB();
    u4 part[4][4];
    unsigned phq[20];
    {
        unsigned qq[10];
        asm volatile("s_nop 6" : "+v"(aq[0][0]), "+v"(aq[1][0]), "+v"(aq[0][1]), "+v"(aq[1][1]), "+v"(aq[0][2]), "+v"(aq[1][2]));
#pragma unroll
        for (int t = 0; t < 3; ++t)
#pragma unroll
            for (int e = 0; e < 2; ++e) {
                const auto sw2 = __builtin_amdgcn_permlane16_swap(pack2(aq[0][t][2 * e], aq[0][t][2 * e + 1]), pack2(aq[1][t][2 * e], aq[1][t][2 * e + 1]), false, false);
                qq[4 * t + e] = sw2[0];
                if (t < 2) qq[4 * t + 2 + e] = sw2[1];
            }
        feature_map(pwq, qq, phq, [&](auto n) {
            if constexpr (n == GATHER_HOOK) {
                wait_flags(fl1, epoch, fv);
                STAMP(6);
#pragma unroll
                for (int bi = 0; bi < 4; ++bi) {
                    const unsigned blk = (bi == 3 && w == 3) ? 14 : w + 4 * bi;
#pragma unroll
                    for (int st = 0; st < 4; ++st)
                        part[bi][st] = __builtin_amdgcn_raw_buffer_load_b128(rws, g * 3840 + i * 16, WS_KVP + (hg * 16 + 4 * st) * 3840 + blk * 256, 16);
                }
            }
            if constexpr (n == 4) static_for<25 - WO_IN_Q>([&](auto m) { dma_wo(std::integral_constant<int, WO_IN_Q + m>{}); });
        });
#pragma unroll
        for (int m = 0; m < 20; m += 4) asm volatile("" : "+v"(phq[m]), "+v"(phq[m + 1]), "+v"(phq[m + 2]), "+v"(phq[m + 3]));
    }
    SB();
    STAMP(7);
    {
        b8 sel[2];
#pragma unroll
        for (int e = 0; e < 2; ++e) {
            u4 v;
#pragma unroll
            for (int m = 0; m < 4; ++m) v[m] = i == 8 * e + 2 * m ? 0x00003f80u : i == 8 * e + 2 * m + 1 ? 0x3f800000u : 0u;
            sel[e] = __builtin_bit_cast(b8, v);
        }
        asm volatile("s_nop 1" : "+v"(sel[0]), "+v"(sel[1]));
        f4 sum[2];
        static_for<2>([&](auto pr) { static_for<8>([&](auto n) { mfma16_vv(n == 0, sum[pr], sel[n / 4], part[2 * pr + n / 4][n % 4]); }); });
        asm volatile("" : "+v"(sum[0]));
        *(LAS u2*)(lds + kvt_dst[0]) = (u2){pack2(sum[0][0], sum[0][1]), pack2(sum[0][2], sum[0][3])};
        asm volatile("s_nop 4" : "+v"(sum[1]) :: "memory");
        *(LAS u2*)(lds + kvt_dst[1]) = (u2){pack2(sum[1][0], sum[1][1]), pack2(sum[1][2], sum[1][3])};
    }
    WAIT_VM_BAR(0);
    STAMP(8);
    {
        b8 ka[5], kb[5];
#pragma unroll
        for (int st = 0; st < 5; ++st) { kb[st] = *(LAS b8*)(lds + k1a + st * k1s); ka[st] = *(LAS b8*)(lds + k0a + st * k0s); }
        SB();
        f16v n0, n1;
        static_for<5>([&](auto st) {
            const b8 bq = as_frag(phq[4 * st], phq[4 * st + 1], phq[4 * st + 2], phq[4 * st + 3]);
            mfma32_pad(st == 0, n1, kb[st], bq);
        });
        static_for<5>([&](auto st) {
            const b8 bq = as_frag(phq[4 * st], phq[4 * st + 1], phq[4 * st + 2], phq[4 * st + 3]);
            mfma32_pad(st == 0, n0, ka[st], bq);
        });
        asm volatile("" : "+v"(n1));
        float inv = __builtin_amdgcn_rcpf(n1[4] + 1e-6f);
        unsigned o[10];
        o[8] = pack2(n1[0] * inv, n1[1] * inv);
        o[9] = pack2(n1[2] * inv, n1[3] * inv);
        const unsigned ob = WS_O + (s * 8 + 2 * w) * 20480 + hg * 1280;
        const unsigned vo = (c >> 4) * 20480 + i * 16;
        __builtin_amdgcn_raw_buffer_store_b64((u2){o[8], o[9]}, rws, vo + 512 + h * 8, ob, 16);
        asm volatile("s_nop 3" : "+v"(n0), "+v"(inv) :: "memory");
#pragma unroll
        for (int m = 0; m < 8; ++m) o[m] = pack2(n0[2 * m] * inv, n0[2 * m + 1] * inv);
        __builtin_amdgcn_raw_buffer_store_b128((u4){o[0], o[1], o[2], o[3]}, rws, vo + h * 768, ob, 16);
        __builtin_amdgcn_raw_buffer_store_b128((u4){o[4], o[5], o[6], o[7]}, rws, vo + h * 768 + 256, ob, 16);
        SB();
        asm volatile("s_nop 1" ::: "memory");
        SB();
    }
    STAMP(9);

    LAS char* const word = lds + L_WO + (16 * h + 4 * g1 + q) * 160 + 8 * p;
    auto o_frag = [&](int s2, int t) { return tr_frag(word + s2 * 5120 + 32 * t, word + s2 * 5120 + 1280 + 32 * t); };
    b8 wf[20][5];
    static_for<16>([&](auto s2) {
        if (s2 == PRE_BEFORE_FLAG) {
            WAIT_VM(0);
            if (l == 0) set_flag((GAS u64*)(ws + WS_FL2 + ((s * 4 + w) * 16 + hg) * 128), epoch);
            STAMP(10);
        }
#pragma unroll
        for (int t = 0; t < 5; ++t) wf[s2][t] = o_frag(s2, t);
        SB();
    });
    const GAS u64* const fl2 = (const GAS u64*)(ws + WS_FL2 + ((rt * 2 + (w >> 1)) * 16 + i) * 128);
    wait_flags(fl2, epoch, load_flag(fl2));
    STAMP(11);
    u4 of[20];
#pragma unroll
    for (int s2 = 0; s2 < 20; ++s2) of[s2] = __builtin_amdgcn_raw_buffer_load_b128(rws, l * 16, WS_O + (rt * 4 + w) * 20480 + s2 * 1024, 16);
    SB();
    f4 ao[5];
    static_for<20>([&](auto s2c) {
        constexpr int s2 = s2c;
        static_for<5>([&](auto t) {
            if constexpr (s2 >= 13 && s2 < 17) wf[s2 + 3][t] = o_frag(s2 + 3, t);
            if constexpr (s2 < 12) mfma16_av(s2 == 0, ao[t], wf[s2][t], of[s2]);
            else mfma16_vv(false, ao[t], wf[s2][t], of[s2]);
            SB();
        });
    });
    STAMP(12);
    {
        GAS char* const orow = (GAS char*)out + (rt * 64 + 16 * w + i) * 1280 + ct * 160;
        asm volatile("s_nop 3" : "+v"(ao[0]), "+v"(ao[1]));
        static_for<2>([&](auto tc) {
            constexpr int t = 2 * tc;
            if (t == 2) { SB(); pin(ao[2], ao[3], ao[4]); }
            const auto lo = __builtin_amdgcn_permlane16_swap(pack2(ao[t][0], ao[t][1]), pack2(ao[t + 1][0], ao[t + 1][1]), false, false);
            const auto hi = __builtin_amdgcn_permlane16_swap(pack2(ao[t][2], ao[t][3]), pack2(ao[t + 1][2], ao[t + 1][3]), false, false);
            *(GAS u4*)(orow + 32 * t + 32 * g1 + 16 * h) = (u4){lo[0], hi[0], lo[1], hi[1]};
        });
        *(GAS u2*)(orow + 128 + 8 * g) = (u2){pack2(ao[4][0], ao[4][1]), pack2(ao[4][2], ao[4][3])};
    }
    if (b == 0 && threadIdx.x == 0) set_flag((GAS u64*)(ws + WS_EPOCH), epoch);
}

__global__ void k_pre(char* ws) { if (threadIdx.x == 0) *(GAS u64*)(ws + (16u << 20)) = __builtin_amdgcn_s_memrealtime(); }
__global__ __launch_bounds__(256, 1) void k_mid(char* ws) {
    const u64 t = __builtin_amdgcn_s_memrealtime();
    const u64 until = t + (t - *(const GAS u64*)(ws + (16u << 20))) * 20;
    while (__builtin_amdgcn_s_memrealtime() < until) __builtin_amdgcn_s_sleep(8);
}

extern "C" void kernel_launch(void* const* d_in, const int* in_sizes, int n_in, void* d_out, int out_size, void* d_ws,
                              size_t ws_size, hipStream_t stream) {
    k_pre<<<1, 64, 0, stream>>>((char*)d_ws);
    k_mid<<<256, 256, 0, stream>>>((char*)d_ws);
    hedgehog_fused<<<256, 256, 0, stream>>>((const char*)d_in[0], (const char*)d_in[1], (const char*)d_in[2],
                                            (const char*)d_in[3], (char*)d_out, (char*)d_ws);
}
```

```cpp
#include <hip/hip_runtime.h>
#include <hip/hip_bf16.h>
#include <utility>

#define DI __device__ __forceinline__
#define GAS __attribute__((address_space(1)))
#define LAS __attribute__((address_space(3)))
#define CAS __attribute__((address_space(4)))
typedef unsigned long long u64;
typedef unsigned u2 __attribute__((ext_vector_type(2)));
typedef unsigned u4 __attribute__((ext_vector_type(4)));
typedef short s4 __attribute__((ext_vector_type(4)));
typedef short s8 __attribute__((ext_vector_type(8)));
typedef __bf16 b2 __attribute__((ext_vector_type(2)));
typedef __bf16 b8 __attribute__((ext_vector_type(8)));
typedef float f2 __attribute__((ext_vector_type(2)));
typedef float f4 __attribute__((ext_vector_type(4)));
typedef float f16v __attribute__((ext_vector_type(16)));

#define DIAG 0

constexpr unsigned WS_EPOCH = 0;
constexpr unsigned WS_FL1 = 4096;
constexpr unsigned WS_FL2 = 65536;
constexpr unsigned WS_KVP = 262144;
constexpr unsigned WS_O = 1310720;
constexpr unsigned WS_END = WS_O + 2048 * 640 * 2;
constexpr unsigned WS_DIAG = 8u << 20;

constexpr unsigned L_WK = 0;
constexpr unsigned L_WV = 51200;
constexpr unsigned L_WO = 51200;
constexpr unsigned L_XR = 102400;
constexpr unsigned L_WPHI = 153600;
constexpr unsigned L_ZERO = 157696;
constexpr unsigned L_JUNK = 158720;
constexpr unsigned L_EPOCH = 158976;
constexpr unsigned L_KVT = 159232;
constexpr unsigned L_SIZE = 163840;

constexpr unsigned SPIN_CAP = 1u << 21;
constexpr unsigned KERNEL_BYTES = 20480;
#define ABL 0
#define ROT_EXPR (3 * s + hg)
#define QX 0
#define POLL_IT 17
#define MAP 1
#define WV7 1
#define WV8 2
#define WV9 2
#define FLAG1_IT 10
#define WO_IN_Q 17
#define GATHER_HOOK 0
#define PRE_BEFORE_FLAG 6

DI u4 make_srd(const void* p, unsigned bytes) {
    const u64 a = (u64)p;
    return (u4){(unsigned)a, (unsigned)(a >> 32) & 0xffffu, bytes, 0x00020000u};
}
DI void set_m0(unsigned lds) { asm volatile("s_mov_b32 m0, %0\n\ts_nop 0" :: "s"(lds) : "memory"); }
DI void set_m0_early(unsigned lds) { asm volatile("s_mov_b32 m0, %0" :: "s"(lds) : "memory"); }
template <int OFF> DI void dma(u4 srd, unsigned soff, unsigned voff) {
    asm volatile("buffer_load_dwordx4 %2, %0, %1 offen offset:%3 lds" :: "s"(srd), "s"(soff), "v"(voff), "i"(OFF) : "memory");
}
template <int PH, bool RESUME = false> DI void dma_piece(unsigned lds, u4 srd, unsigned soff, unsigned voff) {
    if (PH == 0 || (RESUME && PH < 4)) set_m0(lds);
    if (PH == 4) set_m0(lds + 4096);
    dma<(PH & 3) * 1024>(srd, soff, voff);
}
DI void touch(unsigned lds, u4 srd, unsigned voff) {
    asm volatile("s_mov_b32 m0, %0\n\ts_nop 0\n\tbuffer_load_dword %2, %1, 0 offen lds" :: "s"(lds), "s"(srd), "v"(voff) : "memory");
}
template <class F, int... I> DI void static_for_impl(F&& f, std::integer_sequence<int, I...>) { (f(std::integral_constant<int, I>{}), ...); }
template <int N, class F> DI void static_for(F&& f) { static_for_impl(f, std::make_integer_sequence<int, N>{}); }
#define SB() __builtin_amdgcn_sched_barrier(0)
#define WAIT_VM(n) asm volatile("s_waitcnt vmcnt(" #n ")" ::: "memory")
#define WAIT_VM_BAR(n) asm volatile("s_waitcnt vmcnt(" #n ") lgkmcnt(0)\n\ts_barrier" ::: "memory")
#define WAIT_LGKM() asm volatile("s_waitcnt lgkmcnt(0)" ::: "memory")
#define LGKM_BAR() asm volatile("s_waitcnt lgkmcnt(0)\n\ts_barrier" ::: "memory")

DI s4 tr_read(LAS char* p) { return __builtin_amdgcn_ds_read_tr16_b64_v4i16((LAS s4*)p); }
DI b8 tr_frag(LAS char* p0, LAS char* p1) {
    const s4 lo = tr_read(p0), hi = tr_read(p1);
    return __builtin_bit_cast(b8, __builtin_shufflevector(lo, hi, 0, 1, 2, 3, 4, 5, 6, 7));
}
DI b8 join(s4 lo, s4 hi) { return __builtin_bit_cast(b8, __builtin_shufflevector(lo, hi, 0, 1, 2, 3, 4, 5, 6, 7)); }
template <int N> using ic = std::integral_constant<int, N>;
DI unsigned pack2(float a, float b) { return __builtin_bit_cast(unsigned, __builtin_convertvector((f2){a, b}, b2)); }
DI b8 as_frag(unsigned a, unsigned b, unsigned c, unsigned d) { return __builtin_bit_cast(b8, (u4){a, b, c, d}); }
#define DEF_MFMA16(name, ca, cb)                                                                                          \
    template <class A, class B> DI void name(bool first, f4& acc, const A& a, const B& b) {                              \
        if (first) asm volatile("v_mfma_f32_16x16x32_bf16 %0, %1, %2, 0" : "=&v"(acc) : ca(a), cb(b));                    \
        else asm volatile("v_mfma_f32_16x16x32_bf16 %0, %1, %2, %0" : "+v"(acc) : ca(a), cb(b));                          \
    }
DEF_MFMA16(mfma16_vv, "v", "v")
DEF_MFMA16(mfma16_va, "v", "a")
DEF_MFMA16(mfma16_av, "a", "v")
DI void mfma32_pad(bool first, f16v& acc, const b8& a, const b8& b) {
    if (first) asm volatile("s_nop 1\n\tv_mfma_f32_32x32x16_bf16 %0, %1, %2, 0" : "=&v"(acc) : "v"(a), "v"(b));
    else asm volatile("s_nop 1\n\tv_mfma_f32_32x32x16_bf16 %0, %1, %2, %0" : "+v"(acc) : "v"(a), "v"(b));
}
DI void mfma32(bool first, f16v& acc, const b8& a, const b8& b) {
    if (first) asm volatile("v_mfma_f32_32x32x16_bf16 %0, %1, %2, 0" : "=&v"(acc) : "v"(a), "v"(b));
    else asm volatile("v_mfma_f32_32x32x16_bf16 %0, %1, %2, %0" : "+v"(acc) : "v"(a), "v"(b));
}
DI void mfma32k8_pad(f16v& acc, const s4& a, const s4& b) {
    asm volatile("s_nop 1\n\tv_mfma_f32_32x32x8_bf16 %0, %1, %2, %0" : "+v"(acc) : "v"(a), "v"(b));
}
template <class T> DI void pin(T& a) { asm volatile("" : "+v"(a)); }
template <class T, class... R> DI void pin(T& a, R&... r) { pin(a); pin(r...); }
DI void landed6(const s4 (&a)[3], const s4 (&b)[3]) { asm volatile("" :: "v"(a[0]), "v"(a[1]), "v"(a[2]), "v"(b[0]), "v"(b[1]), "v"(b[2])); }
DI void landed(const b8& a, const b8& b) { asm volatile("" :: "a"(a), "a"(b) : "memory"); }
template <int N> DI void wait_vm_bar() { asm volatile("s_waitcnt vmcnt(%0) lgkmcnt(0)\n\ts_barrier" :: "i"(N) : "memory"); }
template <int N> DI void wait_vm_only_bar() { asm volatile("s_waitcnt vmcnt(%0)\n\ts_barrier" :: "i"(N) : "memory"); }
template <int N> DI void wait_vm() { asm volatile("s_waitcnt vmcnt(%0)" :: "i"(N) : "memory"); }

struct PhiW { b8 a0[2], a1[2]; s4 a2[2]; };

template <int T> DI void to_lane_rows(const f4 (&a)[2][3], unsigned (&r)[12]) {
#pragma unroll
    for (int e = 0; e < 2; ++e) {
        const auto sw = __builtin_amdgcn_permlane16_swap(pack2(a[0][T][2 * e], a[0][T][2 * e + 1]), pack2(a[1][T][2 * e], a[1][T][2 * e + 1]), false, false);
        r[4 * T + e] = sw[0];
        r[4 * T + 2 + e] = sw[1];
    }
}
DI f2 exp2_pair(f2 x, float scale, float bias) {
    const f2 a = __builtin_elementwise_fma(x, (f2){scale, scale}, (f2){bias, bias});
    return (f2){__builtin_amdgcn_exp2f(a[0]), __builtin_amdgcn_exp2f(a[1])};
}
DI float sum20(const f2 (&e)[10]) {
    const f2 t = ((e[0] + e[1]) + (e[2] + e[3])) + ((e[4] + e[5]) + (e[6] + e[7])) + (e[8] + e[9]);
    return t[0] + t[1];
}
template <bool NORM, class F> DI float feature_map(const PhiW& w, f4 (&a)[2][3], unsigned (&out)[20], F&& hook) {
    unsigned in[12];
    f16v d0, d1;
    pin(a[0][0], a[1][0]);
    to_lane_rows<0>(a, in);
    const b8 b0 = as_frag(in[0], in[1], in[2], in[3]);
    mfma32_pad(true, d0, w.a0[0], b0);
    mfma32_pad(true, d1, w.a0[1], b0);
    pin(a[0][1], a[1][1], a[0][2], a[1][2]);
    to_lane_rows<1>(a, in);
    const b8 b1 = as_frag(in[4], in[5], in[6], in[7]);
    mfma32_pad(false, d0, w.a1[0], b1);
    mfma32_pad(false, d1, w.a1[1], b1);
    SB();
    to_lane_rows<2>(a, in);
    const s4 b2_ = __builtin_bit_cast(s4, (u2){in[8], in[9]});
    mfma32k8_pad(d0, w.a2[0], b2_);
    mfma32k8_pad(d1, w.a2[1], b2_);
    SB(); hook(ic<0>{}); SB();
    asm volatile("s_nop 7" : "+v"(d0));
    float p[20];
#pragma unroll
    for (int r = 0; r < 16; ++r) p[r] = d0[r];
    float mx = p[0], mn = p[0];
#pragma unroll
    for (int r = 1; r < 15; ++r) { mx = __builtin_elementwise_maximum(mx, p[r]); mn = __builtin_elementwise_minimum(mn, p[r]); }
    pin(mx, mn); pin(d1);
#pragma unroll
    for (int r = 16; r < 20; ++r) p[r] = d1[r - 16];
#pragma unroll
    for (int r = 15; r < 20; ++r) { mx = __builtin_elementwise_maximum(mx, p[r]); mn = __builtin_elementwise_minimum(mn, p[r]); }
    constexpr float L = 1.4426950408889634f;
    const float cp = -mx * L, cn = mn * L;
    f2 e[10], f[10];
    SB(); hook(ic<1>{}); SB();
#pragma unroll
    for (int m = 0; m < 10; ++m) e[m] = exp2_pair((f2){p[2 * m], p[2 * m + 1]}, L, cp);
    const float se = sum20(e);
    SB(); hook(ic<2>{}); SB();
    const float re = NORM ? __builtin_amdgcn_rcpf(se) : 1.f;
#pragma unroll
    for (int m = 0; m < 5; ++m) f[m] = exp2_pair((f2){p[2 * m], p[2 * m + 1]}, -L, cn);
#pragma unroll
    for (int m = 0; m < 10; ++m) { const f2 t = NORM ? e[m] * (f2){re, re} : e[m]; out[m] = pack2(t[0], t[1]); }
    SB(); hook(ic<3>{}); SB();
#pragma unroll
    for (int m = 5; m < 10; ++m) f[m] = exp2_pair((f2){p[2 * m], p[2 * m + 1]}, -L, cn);
    const float sf = __builtin_amdgcn_rcpf(sum20(f)), rf = NORM ? sf : sf * se;
    SB(); hook(ic<4>{}); SB();
    static_for<10>([&](auto m) { hook(ic<10 + m>{}); const f2 t = f[m] * (f2){rf, rf}; out[10 + m] = pack2(t[0], t[1]); SB(); });
    hook(ic<5>{}); SB();
    return se;
}

DI u64 load_flag(const GAS u64* p) { return __hip_atomic_load(p, __ATOMIC_RELAXED, __HIP_MEMORY_SCOPE_AGENT); }
DI void wait_flags(const GAS u64* p, u64 epoch, u64 v) {
    if (!__all(v == epoch)) {
        u64 v0 = load_flag(p);
        __builtin_amdgcn_s_sleep(4);
#pragma unroll 1
        for (unsigned spins = 0; spins < SPIN_CAP; ++spins) {
            const u64 v1 = load_flag(p);
            if (__all(v0 == epoch)) break;
            v0 = load_flag(p);
            if (__all(v1 == epoch)) break;
        }
    }
    asm volatile("" ::: "memory");
}
DI void set_flag(GAS u64* p, u64 epoch) { __hip_atomic_store(p, epoch, __ATOMIC_RELAXED, __HIP_MEMORY_SCOPE_AGENT); }

#define STAMP(i) do { } while (0)

__global__ __launch_bounds__(256, 1) void hedgehog_fused(const char* __restrict__ x, const char* __restrict__ wqkv,
                                                         const char* __restrict__ wphi, const char* __restrict__ wo,
                                                         char* __restrict__ out, char* __restrict__ ws) {
    __shared__ __attribute__((aligned(16))) char lds_raw[L_SIZE];
    LAS char* const lds = (LAS char*)lds_raw;
    const unsigned lb = (unsigned)(size_t)lds;
    STAMP(0);
    const unsigned b = blockIdx.x, xcd = b & 7, r = b >> 3;
    {
        u64 pc;
        asm volatile("s_getpc_b64 %0" : "=s"(pc));
        if (!(ABL & 128) && threadIdx.x < 8) touch(lb + L_JUNK, make_srd((const void*)(pc & ~127ull), KERNEL_BYTES), (r * 8 + threadIdx.x) * 128);
    }
    SB();
    const unsigned l = threadIdx.x & 63, w = __builtin_amdgcn_readfirstlane(threadIdx.x >> 6);
    const unsigned g = l >> 4, i = l & 15, h = l >> 5, c = l & 31, q = (l >> 2) & 3, p = l & 3, g1 = g & 1;
    const unsigned s = MAP == 1 ? (xcd & 1) * 8 + (r & 7) : (xcd & 3) * 4 + (r & 3);
    const unsigned hg = MAP == 1 ? (xcd >> 1) * 4 + (r >> 3) : (xcd >> 2) * 8 + (r >> 2);
    const unsigned hq = hg ^ QX;
    const unsigned rt = xcd * 4 + (r & 3), ct = r >> 2;
    const unsigned rot = (ROT_EXPR) % 10;
    auto ktile = [&](unsigned j) { const unsigned k = rot + j; return k >= 10 ? k - 10 : k; };

    unsigned xo[4], wo_[5];
#pragma unroll
    for (int m = 0; m < 4; ++m) {
        const unsigned row = 8 * m + (l >> 3);
        xo[m] = (s * 128 + 32 * w + row) * 1280 + (((l & 7) ^ ((row >> 1) & 7)) << 4) - 1024 * m;
    }
    asm volatile("" :: "s"(x), "s"(wqkv), "s"(wphi), "s"(wo), "s"(out), "s"(ws));
    STAMP(15);
    const u4 sx = make_srd(x, 2048 * 1280), sw = make_srd(wqkv, 640 * 3840), sp = make_srd(wphi, 32 * 800), so = make_srd(wo, 640 * 1280);
    const unsigned xr = L_XR + w * 12288;
    auto dma_x = [&](unsigned j, auto mc) { dma_piece<mc>(lb + xr + (j % 3) * 4096, sx, ktile(j) * 128, xo[mc]); };
    static_for<4>([&](auto m) { dma_x(0, m); });
    static_for<4>([&](auto m) { dma_x(1, m); });
    STAMP(16);
    SB();
#pragma unroll
    for (int m = 0; m < 5; ++m) {
        const unsigned ph = (w + 4 * m) % 5;
        const unsigned n = 64 * ph + l, u = n / 5, ch = n - 5 * u;
        const unsigned row = (u & 56) + 4 * (u & 1) + ((u >> 1) & 3);
        wo_[m] = row * 3840 + ch * 16;
    }
    auto w_dst = [&](unsigned dst, unsigned n) { const unsigned m = 4 * n + w, pr = m / 5; return lb + dst + (pr > 9 ? 9 : pr) * 5120 + (m - 5 * pr) * 1024; };
    auto w_src = [&](unsigned col, unsigned n) { const unsigned pr = (4 * n + w) / 5; return ktile(pr > 9 ? 9 : pr) * 245760 + col; };
    const unsigned colq = hq * 80, colk = 1280 + hg * 80, colv = 2560 + hg * 80;
    auto dma_w = [&](unsigned dst, unsigned col, auto nc) { dma_piece<0>(w_dst(dst, nc), sw, w_src(col, nc), wo_[nc % 5]); };
    STAMP(17);
    static_for<3>([&](auto n) { dma_w(L_WK, colk, n); });
    SB();
    if (w == 3) *(LAS u4*)(lds + L_ZERO + l * 16) = (u4){0, 0, 0, 0};

    unsigned kvt_dst[2];
#pragma unroll
    for (int pr = 0; pr < 2; ++pr) {
        const unsigned bi = (2 * pr + h + s) & 3, blk = (bi == 3 && w == 3) ? 14 : w + 4 * bi;
        const unsigned ci = 16 * blk + i, hh = ci >= 120, cc = ci - 120 * hh;
        const bool t1 = cc >= 80;
        const unsigned m = t1 ? cc - 80 : cc, sh = t1 ? 3 : 4, nfull = t1 ? 32 : 64;
        const bool f2_ = m >= nfull;
        const unsigned idx = f2_ ? 2 * (m - nfull) + g1 : m;
        const unsigned d = (t1 ? 16 : 0) + (idx & ((1u << sh) - 1)), f0 = (f2_ ? 32 : 16 * g1) + 4 * (idx >> sh);
        kvt_dst[pr] = L_KVT + hh * 1920 + d * 80 + f0 * 2;
    }
    unsigned woo[5];
#pragma unroll
    for (int ph = 0; ph < 5; ++ph) {
        const unsigned n = 64 * ph + l, u = n / 10, ch = n - 10 * u;
        const unsigned row = 16 * (u >> 4) + 8 * ((u >> 2) & 1) + 4 * ((u >> 3) & 1) + (u & 3);
        woo[ph] = row * 1280 + ct * 160 + ch * 16 - (ph < 4 ? 1024 * ph : 0);
    }
    unsigned kva[2], pha[2][2], gto[4];
    {
        const unsigned a_ = c >> 3, hv = (c >> 2) & 1, row = L_KVT + h * 1920 + (4 * a_ + (c & 3)) * 80;
        kva[0] = hv == h ? row : L_ZERO;
        kva[1] = (hv == h && a_ < 2) ? row + 1280 : L_ZERO;
        const unsigned a2 = 2 * g1 + (p >> 1);
#pragma unroll
        for (int t = 0; t < 2; ++t) {
            const unsigned base = ((p & 1) == h && (t == 0 || a2 == 0)) ? L_WPHI + h * 800 + q * 40 + (16 * t + 4 * a2) * 2 : L_ZERO - 1600;
            pha[t][0] = base + h * 160;
            pha[t][1] = base + (h ? 0 : 640);
        }
    }
#pragma unroll
    for (int bi = 0; bi < 4; ++bi) { const unsigned br = (bi + s) & 3; gto[bi] = g * 3840 + i * 16 + ((br == 3 && w == 3) ? 14 : w + 4 * br) * 256; }
    u4 sel[2];
#pragma unroll
    for (int m = 0; m < 8; ++m) sel[m >> 2][m & 3] = (i >> 1) == m ? 0x3f80u << (16 * (i & 1)) : 0u;
    unsigned xrd0 = xr + i * 128 + ((g ^ (i >> 1)) << 4), xrd1 = xr + i * 128 + (((4 + g) ^ (i >> 1)) << 4);
    const unsigned wrow = (16 * h + 2 * (4 * g1 + q)) * 80;
    unsigned wrd[2][2];
    wrd[0][0] = wrow + (p < 2 ? 8 * p : 32 + 8 * p); wrd[0][1] = wrow + ((p == 1 || p == 2) ? 40 : 32);
    wrd[1][0] = wrd[0][0] + L_WV; wrd[1][1] = wrd[0][1] + L_WV;
    pin(kvt_dst[0], kvt_dst[1], kva[0], kva[1], pha[0][0], pha[0][1], pha[1][0], pha[1][1]);
    pin(woo[0], woo[1], woo[2], woo[3], woo[4], xrd0, xrd1, wrd[0][0], wrd[0][1], wrd[1][0], wrd[1][1]);
    pin(sel[0], sel[1]);
    pin(gto[0], gto[1], gto[2], gto[3]);
    SB();
    STAMP(18);
    dma_piece<0>(lb + L_WPHI + w * 1024, sp, (hg >> 1) * 3200 + w * 1024, l * 16);
    touch(lb + (w == 0 ? L_EPOCH : L_JUNK), make_srd(ws, WS_END), l < 2 ? WS_EPOCH + 4 * l : (l & 3) << 20);
    static_for<4>([&](auto m) { dma_x(2, m); });
    static_for<2>([&](auto n) { dma_w(L_WK, colk, ic<3 + n>{}); });
    STAMP(19);
    SB();
    auto dma_wo = [&](auto nc) { if (ABL & 1) return; const unsigned s2 = 4 * (nc / 5) + w; dma_piece<nc % 5, true>(lb + L_WO + s2 * 5120, so, s2 * 40960, woo[nc % 5]); };

    unsigned phb[2][2][2];
#pragma unroll
    for (int set = 0; set < 2; ++set)
#pragma unroll
        for (int t = 0; t < 2; ++t)
#pragma unroll
            for (int e = 0; e < 2; ++e) phb[set][t][e] = pha[t][e] + (pha[t][e] >= L_ZERO - 1600 ? 1600 : ((set ? hq : hg) & 1) * 1600);

    b8 xf[20][2];
    s4 wl[3][3], wh[3][3], pf[2][2][5];
    u2 ew;
    auto x_read = [&](int it, int t) { return *(LAS b8*)(lds + ((it & 1) ? xrd1 : xrd0) + ((it >> 1) % 3) * 4096 + t * 2048); };
    auto w_read = [&](int part, int it, int t, int hf) { return tr_read(lds + (t < 2 ? wrd[part][0] + 16 * t : wrd[part][1]) + it * 2560 + hf * 80); };

    f4 ak[2][3];
    wait_vm_bar<6>();
    STAMP(1);
    xf[0][0] = x_read(0, 0); xf[0][1] = x_read(0, 1);
#pragma unroll
    for (int t = 0; t < 3; ++t) { wl[0][t] = w_read(0, 0, t, 0); wh[0][t] = w_read(0, 0, t, 1); SB(); }
    xf[1][0] = x_read(1, 0); xf[1][1] = x_read(1, 1);
    SB();
    static_for<10>([&](auto jc) {
        constexpr int j = jc;
        constexpr int NW[9] = {63, 10, 11, 9, 10, 10, 11, 4, WV7 + WV8};
        constexpr int K0[7] = {5, 7, 8, 9, 10, 12, 13};
        constexpr int XM = j == 8 ? 8 : 6;
        static_for<12>([&](auto mc) {
            constexpr int m = mc, k2 = m / 6, t = (m % 6) / 2, hf = m & 1, it = 2 * j + k2;
            if constexpr (m == XM && j > 0 && j < 9) { if (j & 1) wait_vm_only_bar<NW[j < 9 ? j : 0]>(); else wait_vm<NW[j < 9 ? j : 0]>(); }
            if constexpr (it < 19) { if (hf) wh[k2 ^ 1][t] = w_read(0, it + 1, t, 1); else wl[k2 ^ 1][t] = w_read(0, it + 1, t, 0); }
            if constexpr (j < 9 && m >= XM && m < XM + 4) xf[2 * j + 2 + (m - XM) / 2][hf] = x_read(2 * j + 2 + (m - XM) / 2, hf);
            if constexpr (j >= 1 && j < 5 && m < 5) {
                constexpr int set = (j - 1) >> 1, tt = (j - 1) & 1;
                pf[set][tt][m] = tr_read(lds + (m < 4 ? phb[set][tt][0] + m * 160 : phb[set][tt][1]));
            }
            if constexpr (j == 5 && m == 0) ew = *(LAS u2*)(lds + L_EPOCH);
            mfma16_va(it == 0, ak[hf][t], join(wl[k2][t], wh[k2][t]), xf[it][hf]);
            if (!(ABL & 16)) {
                if constexpr (j < 7 && m >= 1 && m < 5) { if (m == 1) landed(xf[2 * j + 1][0], xf[2 * j + 1][1]); dma_x(j + 3, ic<(m - 1) & 3>{}); }
                if constexpr (j < 6 && (m == 7 || m == 10)) { if constexpr (K0[j] + (m == 10) < K0[j + 1]) dma_w(L_WK, colk, ic<K0[j < 6 ? j : 0] + (m == 10)>{}); }
                if constexpr (j == 7 && m >= 7 && m < 7 + WV7) dma_w(L_WV, colv, ic<m - 7>{});
                if constexpr (j == 8 && m % 3 == 1 && m / 3 < WV8) dma_w(L_WV, colv, ic<WV7 + m / 3>{});
                if constexpr (j == 9 && m % 3 == 1 && m / 3 < WV9) dma_w(L_WV, colv, ic<WV7 + WV8 + m / 3>{});
            }
            SB();
        });
    });
    const u64 epoch = (((u64)(unsigned)__builtin_amdgcn_readfirstlane(ew[1]) << 32) | (unsigned)__builtin_amdgcn_readfirstlane(ew[0])) * 6364136223846793005ull + 1442695040888963407ull;
    PhiW pwk, pwq;
#pragma unroll
    for (int t = 0; t < 2; ++t) {
        pwk.a0[t] = join(pf[0][t][0], pf[0][t][1]); pwk.a1[t] = join(pf[0][t][2], pf[0][t][3]); pwk.a2[t] = pf[0][t][4];
        pwq.a0[t] = join(pf[1][t][0], pf[1][t][1]); pwq.a1[t] = join(pf[1][t][2], pf[1][t][3]); pwq.a2[t] = pf[1][t][4];
    }
    STAMP(2);

    {
        unsigned ph[20];
        LAS u4* const prow = (LAS u4*)(lds + xr + l * 80);
        asm volatile("s_nop 3");
        feature_map<true>(pwk, ak, ph, [&](auto n) {
            constexpr int REST = 13 - WV7 - WV8 - WV9, lo = n < 6 ? (n * REST + 5) / 6 : 0, hi = n < 6 ? ((n + 1) * REST + 5) / 6 : 0;
            static_for<hi - lo>([&](auto e) { dma_w(L_WV, colv, ic<13 - REST + lo + e>{}); });
            if constexpr (n == 3) { prow[0] = (u4){ph[0], ph[1], ph[2], ph[3]}; prow[1] = (u4){ph[4], ph[5], ph[6], ph[7]}; }
            if constexpr (n == 5) {
#pragma unroll
                for (int m = 2; m < 5; ++m) prow[m] = (u4){ph[4 * m], ph[4 * m + 1], ph[4 * m + 2], ph[4 * m + 3]};
            }
        });
    }
    STAMP(3);

    auto project = [&](auto part, f4 (&acc)[2][3], auto&& pre, auto&& dma_a, auto&& dma_b) {
#pragma unroll
        for (int it = 0; it < 2; ++it)
#pragma unroll
            for (int t = 0; t < 3; ++t) { wl[it][t] = w_read(part, it, t, 0); wh[it][t] = w_read(part, it, t, 1); SB(); }
        static_for<20>([&](auto itc) {
            constexpr int it = itc;
            pre(itc);
            if (it > 0) landed6(wl[it % 3], wh[it % 3]);
            static_for<6>([&](auto mc) {
                constexpr int t = mc / 2, hf = mc & 1;
                if constexpr (mc == 2) dma_a(itc);
                mfma16_va(it == 0, acc[hf][t], join(wl[it % 3][t], wh[it % 3][t]), xf[it][hf]);
                if constexpr (mc == 3) dma_b(itc);
                if constexpr (it + 2 < 20) { if (hf) wh[(it + 2) % 3][t] = w_read(part, it + 2, t, 1); else wl[(it + 2) % 3][t] = w_read(part, it + 2, t, 0); }
                SB();
            });
        });
    };

    f4 av[2][3];
    wait_vm_only_bar<8>();
    project(ic<1>{}, av,
            [&](auto it) { if constexpr (it == 6) wait_vm_only_bar<3 + 4>(); if constexpr (it == 14) wait_vm_only_bar<10>();
                           if (ABL & 512) { if constexpr (it == 1) STAMP(21); if constexpr (it == 5) STAMP(22); if constexpr (it == 13) STAMP(23); } },
            [&](auto it) { if constexpr (it % 3 != 2 && it < 19) set_m0_early(w_dst(L_WK, it - it / 3)); },
            [&](auto it) {
                if constexpr (it % 3 != 2 && it < 19) if (!(ABL & 1024)) dma<0>(sw, w_src(colq, it - it / 3), wo_[(it - it / 3) % 5]);
                if constexpr (it == 19) touch(lb + L_JUNK, so, (b * 25 + 7 * w + (l < 7 ? l : 0)) * 128);
            });
    STAMP(4);
    {
        unsigned vv[12];
        LAS u4* const vrow = (LAS u4*)(lds + xr + 5120 + l * 48);
        asm volatile("s_nop 3");
        pin(av[0][0], av[1][0]);
        to_lane_rows<0>(av, vv);
        vrow[0] = (u4){vv[0], vv[1], vv[2], vv[3]};
        SB();
        pin(av[0][1], av[1][1], av[0][2], av[1][2]);
        to_lane_rows<1>(av, vv);
        vrow[1] = (u4){vv[4], vv[5], vv[6], vv[7]};
        SB();
        to_lane_rows<2>(av, vv);
        vrow[2] = (u4){vv[8], vv[9], 0x00003f80u, 0u};
    }
    const __amdgpu_buffer_rsrc_t rws = __builtin_amdgcn_make_buffer_rsrc(ws, 0, WS_END, 0x00020000);
    {
        const unsigned hh = w >> 1, tt = w & 1, rho = hh * 32 + 16 * h + 2 * (4 * g1 + q);
        LAS char* const pa = lds + L_XR + rho * 80 + 8 * p;
        LAS char* const pb = lds + L_XR + 5120 + rho * 48 + 32 * tt + 8 * p;
        b8 fa[4][3], fb[4];
#pragma unroll
        for (int st = 0; st < 4; ++st)
#pragma unroll
            for (int f = 0; f < 3; ++f) fa[st][f] = tr_frag(pa + st * 12288 + 32 * f, pa + st * 12288 + 80 + 32 * f);
        const unsigned rec = WS_KVP + (hg * 16 + s) * 3840 + hh * 1920 + tt * 1280;
        const unsigned slot = tt ? 8 * g + i : l;
        const bool on = tt == 0 || i < 8;
        const unsigned big = 0x7ffffff0u;
        unsigned o0 = on ? slot * 16 : big, o1 = (on && g < 2) ? (tt ? 512 : 1024) + slot * 8 : big;
        pin(o0, o1);
        LGKM_BAR();
#pragma unroll
        for (int st = 0; st < 4; ++st) { fb[st] = tr_frag(pb + st * 12288, pb + st * 12288 + 48); SB(); }
        f4 kv[3];
        static_for<4>([&](auto st) { static_for<3>([&](auto f) { mfma16_vv(st == 0, kv[f], fa[st][f], fb[st]); }); });
        asm volatile("s_nop 5");
        pin(kv[0], kv[1]);
        __builtin_amdgcn_raw_buffer_store_b128((u4){pack2(kv[0][0], kv[0][1]), pack2(kv[0][2], kv[0][3]), pack2(kv[1][0], kv[1][1]), pack2(kv[1][2], kv[1][3])}, rws, o0, rec, 16);
        SB();
        pin(kv[2]);
        __builtin_amdgcn_raw_buffer_store_b64((u2){pack2(kv[2][0], kv[2][1]), pack2(kv[2][2], kv[2][3])}, rws, o1, rec, 16);
    }
    wait_vm_only_bar<2>();
    STAMP(20);

    f4 aq[2][3];
    const GAS u64* const fl1 = (const GAS u64*)(ws + WS_FL1 + (hq * 16 + (l >> 2)) * 128 + (l & 3) * 8);
    u64 fv;
    project(ic<0>{}, aq,
            [&](auto it) {
                if constexpr (it == FLAG1_IT) {
                    wait_vm<FLAG1_IT>();
                    if (l == 0) set_flag((GAS u64*)(ws + WS_FL1 + (hg * 16 + s) * 128 + w * 8), epoch);
                    SB();
                }
                if constexpr (it == POLL_IT) { fv = load_flag(fl1); SB(); }
            },
            [&](auto it) { if constexpr (it < WO_IN_Q && (it % 5 == 0 || it % 5 == 4)) set_m0_early(lb + L_WO + (4 * (it / 5) + w) * 5120 + (it % 5) * 1024); },
            [&](auto it) { if constexpr (it < WO_IN_Q) dma<(it % 5 & 3) * 1024>(so, (4 * (it / 5) + w) * 40960, woo[it % 5]); });
    STAMP(5);
    u4 part[4][4];
    unsigned phq[20];
    f4 sum[2];
    asm volatile("s_nop 2");
    const float sq = feature_map<false>(pwq, aq, phq, [&](auto n) {
        if constexpr (n == GATHER_HOOK) {
            wait_flags(fl1, epoch, fv);
            STAMP(6);
#pragma unroll
            for (int bi = 0; bi < 4; ++bi)
#pragma unroll
                for (int st = 0; st < 4; ++st) part[bi][st] = __builtin_amdgcn_raw_buffer_load_b128(rws, gto[bi], WS_KVP + (hq * 16 + 4 * st) * 3840, 16);
        }
        if constexpr (n >= 10 && n < 18) {
            constexpr int k = n - 10;
            static_for<2>([&](auto e) { constexpr int m = 2 * k + e; mfma16_vv(m % 8 == 0, sum[m / 8], sel[m / 4 % 2], part[m / 4][m % 4]); });
        }
    });
#pragma unroll
    for (int m = 0; m < 20; m += 4) pin(phq[m], phq[m + 1], phq[m + 2], phq[m + 3]);
    SB();
    STAMP(7);
    pin(sum[0]);
    *(LAS u2*)(lds + kvt_dst[0]) = (u2){pack2(sum[0][0], sum[0][1]), pack2(sum[0][2], sum[0][3])};
    pin(sum[1]);
    *(LAS u2*)(lds + kvt_dst[1]) = (u2){pack2(sum[1][0], sum[1][1]), pack2(sum[1][2], sum[1][3])};
    SB();
    static_for<25 - WO_IN_Q>([&](auto e) { dma_wo(ic<WO_IN_Q + e>{}); });
    const unsigned ob = WS_O + (s * 8 + 2 * w) * 20480 + hq * 1280;
    unsigned vo0 = (c >> 4) * 20480 + i * 16 + h * 768, vo1 = (c >> 4) * 20480 + i * 16 + 512 + h * 8;
    float eps = 1e-6f * sq;
    pin(vo0, vo1); pin(eps);
    LGKM_BAR();
    STAMP(8);
    {
        b8 ka[5], kb[5];
#pragma unroll
        for (int st = 0; st < 5; ++st) { kb[st] = *(LAS b8*)(lds + kva[1] + st * 16); SB(); }
#pragma unroll
        for (int st = 0; st < 5; ++st) { ka[st] = *(LAS b8*)(lds + kva[0] + st * 16); SB(); }
        f16v n0, n1;
        static_for<5>([&](auto st) { mfma32(st == 0, n1, kb[st], as_frag(phq[4 * st], phq[4 * st + 1], phq[4 * st + 2], phq[4 * st + 3])); });
        static_for<5>([&](auto st) { mfma32(st == 0, n0, ka[st], as_frag(phq[4 * st], phq[4 * st + 1], phq[4 * st + 2], phq[4 * st + 3])); });
        asm volatile("s_nop 6");
        pin(n1);
        float inv = __builtin_amdgcn_rcpf(n1[4] + eps);
        unsigned o[10];
        o[8] = pack2(n1[0] * inv, n1[1] * inv);
        o[9] = pack2(n1[2] * inv, n1[3] * inv);
        __builtin_amdgcn_raw_buffer_store_b64((u2){o[8], o[9]}, rws, vo1, ob, 16);
        SB();
        pin(inv); pin(n0);
#pragma unroll
        for (int m = 0; m < 4; ++m) o[m] = pack2(n0[2 * m] * inv, n0[2 * m + 1] * inv);
        __builtin_amdgcn_raw_buffer_store_b128((u4){o[0], o[1], o[2], o[3]}, rws, vo0, ob, 16);
        SB();
        asm volatile("s_nop 1" ::: "memory");
        SB();
#pragma unroll
        for (int m = 4; m < 8; ++m) o[m] = pack2(n0[2 * m] * inv, n0[2 * m + 1] * inv);
        __builtin_amdgcn_raw_buffer_store_b128((u4){o[4], o[5], o[6], o[7]}, rws, vo0 + 256, ob, 16);
        SB();
        asm volatile("s_nop 1" ::: "memory");
        SB();
    }
    STAMP(9);

    LAS char* const word = lds + L_WO + (16 * h + 4 * g1 + q) * 160 + 8 * p;
    s4 wfl[20][5], wfh[20][5];
    static_for<16>([&](auto it) {
        if (it == PRE_BEFORE_FLAG) {
            WAIT_VM(0);
            if (l == 0) set_flag((GAS u64*)(ws + WS_FL2 + ((s * 4 + w) * 16 + hq) * 128), epoch);
            asm volatile("s_barrier" ::: "memory");
            STAMP(10);
        }
#pragma unroll
        for (int t = 0; t < 5; ++t) { wfl[it][t] = tr_read(word + it * 5120 + 32 * t); wfh[it][t] = tr_read(word + it * 5120 + 1280 + 32 * t); }
        SB();
    });
    const GAS u64* const fl2 = (const GAS u64*)(ws + WS_FL2 + ((rt * 2 + (w >> 1)) * 16 + i) * 128);
    wait_flags(fl2, epoch, load_flag(fl2));
    STAMP(11);
    u4 of[20];
#pragma unroll
    for (int it = 0; it < 20; ++it) of[it] = __builtin_amdgcn_raw_buffer_load_b128(rws, l * 16 + (it & 3) * 1024, WS_O + (rt * 4 + w) * 20480 + (it >> 2) * 4096, 16);
    SB();
    f4 ao[5];
    static_for<20>([&](auto itc) {
        constexpr int it = itc;
        LAS char* const a = word + (16 + (it / 2 & 3)) * 5120 + (it & 1) * 1280;
        static_for<5>([&](auto t) {
            if constexpr (it < 12) mfma16_av(it == 0, ao[t], join(wfl[it][t], wfh[it][t]), of[it]);
            else mfma16_vv(false, ao[t], join(wfl[it][t], wfh[it][t]), of[it]);
            if constexpr (it < 8) { if (it & 1) wfh[16 + it / 2][t] = tr_read(a + 32 * t); else wfl[16 + it / 2][t] = tr_read(a + 32 * t); }
            SB();
        });
    });
    STAMP(12);
    {
        GAS char* const orow = (GAS char*)out + (rt * 64 + 16 * w + i) * 1280 + ct * 160;
        asm volatile("s_nop 3" : "+v"(ao[0]), "+v"(ao[1]));
        static_for<2>([&](auto tc) {
            constexpr int t = 2 * tc;
            if (t == 2) { SB(); pin(ao[2], ao[3], ao[4]); }
            const auto lo = __builtin_amdgcn_permlane16_swap(pack2(ao[t][0], ao[t][1]), pack2(ao[t + 1][0], ao[t + 1][1]), false, false);
            const auto hi = __builtin_amdgcn_permlane16_swap(pack2(ao[t][2], ao[t][3]), pack2(ao[t + 1][2], ao[t + 1][3]), false, false);
            *(GAS u4*)(orow + 32 * t + 32 * g1 + 16 * h) = (u4){lo[0], hi[0], lo[1], hi[1]};
        });
        *(GAS u2*)(orow + 128 + 8 * g) = (u2){pack2(ao[4][0], ao[4][1]), pack2(ao[4][2], ao[4][3])};
    }
    if (b == 0 && threadIdx.x == 0) set_flag((GAS u64*)(ws + WS_EPOCH), epoch);
}

extern "C" void kernel_launch(void* const* d_in, const int* in_sizes, int n_in, void* d_out, int out_size, void* d_ws,
                              size_t ws_size, hipStream_t stream) {
    hedgehog_fused<<<256, 256, 0, stream>>>((const char*)d_in[0], (const char*)d_in[1], (const char*)d_in[2],
                                            (const char*)d_in[3], (char*)d_out, (char*)d_ws);
}
```
